```python
import torch

def _softmax(x, axis=-1):
    return torch.softmax(x, dim=axis)
def _gelu(x):
    return torch.nn.functional.gelu(x, approximate="tanh")
def _silu(x):
    return torch.nn.functional.silu(x)
def _sigmoid(x):
    return torch.sigmoid(x)
def _layernorm(x, axis=-1, eps=1e-5):
    m = x.mean(dim=axis, keepdim=True)
    v = x.var(dim=axis, keepdim=True, unbiased=False)
    return (x - m) / torch.sqrt(v + eps)
def _scatter_add(tgt, idx, src, axis=0):
    return tgt.clone().index_add_(axis, idx, src)
def _segment_sum(src, seg, n):
    out = torch.zeros((n,) + src.shape[1:], dtype=src.dtype)
    return out.index_add_(0, seg, src)
def _pad(x, pw):
    flat = tuple(v for pair in reversed(pw) for v in pair)
    return torch.nn.functional.pad(x, flat)

kernel_name = 'standalone__gqa_block_cached__near_ridge'

def setup_inputs():

    _w = (lambda a: (lambda t: t.to(torch.bfloat16) if t.is_floating_point() else t)(torch.as_tensor(a)))
    B, Sq, D = 16, 64, 448
    Hq, Hkv, Sk, Dh = 16, 8, 512, 256
    assert Hq % Hkv == 0, (Hq, Hkv)
    return {
        "x": _w((lambda *s: torch.randn(*s, dtype=torch.bfloat16) * 0.8)(B, Sq, D)),
        "k_cache": _w((lambda *s: torch.randn(*s, dtype=torch.bfloat16) * 0.8)(B, Hkv, Sk, Dh)),
        "v_cache": _w((lambda *s: torch.randn(*s, dtype=torch.bfloat16) * 0.05)(B, Hkv, Sk, Dh)),
        "wq": _w((lambda *s: torch.randn(*s, dtype=torch.bfloat16) * 0.8)(D, Hq * Dh) * (D ** -0.5)),
        "wo": _w((lambda *s: torch.randn(*s, dtype=torch.bfloat16) * 0.05)(Hq * Dh, D) * ((Hq * Dh) ** -0.5)),
    }

def reference(x, k_cache, v_cache, wq, wo):
    B, Sq, D = x.shape
    Hq, Dh = 16, 256
    Hkv = k_cache.shape[1]
    rep = Hq // Hkv
    q = (lambda x, *d: x.permute(*d))((x @ wq).reshape(B, Sq, Hq, Dh), 0, 2, 1, 3)
    k = (lambda x, n, axis: torch.repeat_interleave(x, n, dim=axis))(k_cache, rep, axis=1)
    v = (lambda x, n, axis: torch.repeat_interleave(x, n, dim=axis))(v_cache, rep, axis=1)
    scores = torch.einsum("bhqd,bhkd->bhqk", q, k) * (Dh ** -0.5)
    p = _softmax(scores, axis=-1)
    o = torch.einsum("bhqk,bhkd->bhqd", p, v)
    return (lambda x, *d: x.permute(*d))(o, 0, 2, 1, 3).reshape(B, Sq, Hq * Dh) @ wo
```

```cpp
#include <hip/hip_runtime.h>
#include <hip/hip_bf16.h>
typedef unsigned short u16;
__device__ __forceinline__ float bf2f(u16 v){ return __uint_as_float(((unsigned)v)<<16); }
__device__ __forceinline__ u16 f2bf(float f){ unsigned u=__float_as_uint(f); u += 0x7fffu + ((u>>16)&1u); return (u16)(u>>16); }
constexpr int B=16,SQ=64,D=448,HQ=16,HKV=8,SK=512,DH=256;
__global__ void qproj(const u16* x,const u16* wq,u16* q){
  int n = blockIdx.x*256+threadIdx.x; int m = blockIdx.y;
  float acc=0; for(int k=0;k<D;++k) acc += bf2f(x[m*D+k])*bf2f(wq[k*4096+n]);
  q[m*4096+n]=f2bf(acc);
}
__global__ void attn(const u16* q,const u16* kc,const u16* vc,u16* o){
  int qi=blockIdx.x, h=blockIdx.y, b=blockIdx.z; int t=threadIdx.x;
  __shared__ float qs[DH]; __shared__ float p[SK]; __shared__ float red[256];
  qs[t]=bf2f(q[(b*SQ+qi)*4096+h*DH+t]); __syncthreads();
  const u16* K=kc+((size_t)(b*HKV+h/2))*SK*DH; const u16* V=vc+((size_t)(b*HKV+h/2))*SK*DH;
  float mx=-1e30f;
  for(int j=t;j<SK;j+=256){ float s=0; for(int d=0;d<DH;++d) s+=qs[d]*bf2f(K[j*DH+d]); s*=0.0625f; p[j]=s; mx=fmaxf(mx,s);}
  red[t]=mx; __syncthreads();
  for(int s=128;s>0;s>>=1){ if(t<s) red[t]=fmaxf(red[t],red[t+s]); __syncthreads(); }
  mx=red[0]; __syncthreads();
  float sum=0; for(int j=t;j<SK;j+=256){ float e=__expf(p[j]-mx); p[j]=e; sum+=e; }
  red[t]=sum; __syncthreads();
  for(int s=128;s>0;s>>=1){ if(t<s) red[t]+=red[t+s]; __syncthreads(); }
  sum=red[0];
  float acc=0; for(int j=0;j<SK;++j) acc+=p[j]*bf2f(V[j*DH+t]);
  o[(b*SQ+qi)*4096+h*DH+t]=f2bf(acc/sum);
}
__global__ void oproj(const u16* o,const u16* wo,u16* out){
  int n=blockIdx.x*64+threadIdx.x; int m=blockIdx.y;
  float acc=0; for(int k=0;k<4096;++k) acc+=bf2f(o[m*4096+k])*bf2f(wo[k*D+n]);
  out[m*D+n]=f2bf(acc);
}
extern "C" void kernel_launch(void* const* d_in, const int* in_sizes, int n_in, void* d_out, int out_size, void* d_ws, size_t ws_size, hipStream_t stream){
  const u16* x=(const u16*)d_in[0]; const u16* k=(const u16*)d_in[1]; const u16* v=(const u16*)d_in[2];
  const u16* wq=(const u16*)d_in[3]; const u16* wo=(const u16*)d_in[4];
  u16* q=(u16*)d_ws; u16* o=q+1024*4096;
  qproj<<<dim3(16,1024),256,0,stream>>>(x,wq,q);
  attn<<<dim3(SQ,HQ,B),256,0,stream>>>(q,k,v,o);
  oproj<<<dim3(7,1024),64,0,stream>>>(o,wo,(u16*)d_out);
}
```

```cpp
#include <hip/hip_runtime.h>

namespace {

typedef unsigned int u32;
typedef unsigned long long u64;
typedef short s16x4 __attribute__((ext_vector_type(4)));
typedef short s16x8 __attribute__((ext_vector_type(8)));
typedef float f32x16 __attribute__((ext_vector_type(16)));
typedef float f32x2 __attribute__((ext_vector_type(2)));
typedef float f32x4 __attribute__((ext_vector_type(4)));
typedef __bf16 bf16x2 __attribute__((ext_vector_type(2)));
typedef u32 u32x4 __attribute__((ext_vector_type(4)));
#define LDSA __attribute__((address_space(3)))
#define DI __device__ __forceinline__
typedef __amdgpu_buffer_rsrc_t rsrc_t;

constexpr int NSLOT = 19;
constexpr int XCH = NSLOT * 8192;
constexpr int LDS_BYTES = XCH + 8192;
constexpr u32 PART_BYTES = 57344;
constexpr u32 WS_FLAGS = 16u * 16u * PART_BYTES;

enum { KX = 0, KWQ = 1, KK = 2, KV = 3, KWO = 4, KPX = 5 };
constexpr int NSTEP = 22;
constexpr int ST_EXP = 11, ST_PRD = 12, ST_PV0 = 13, ST_LAST = 21;
constexpr int MAXLD = 136;

constexpr int need_step(int kind, int s, int i) {
  return kind == KX ? (i < 4 ? 0 : 1)
       : kind == KWQ ? 1 + 2 * s + (i < 4 ? 0 : 1)
       : kind == KK ? 3 + 2 * s + (i < 4 ? 0 : 1)
       : kind == KV ? ST_PV0 + 2 * s + (i < 4 ? 0 : 1)
       : kind == KWO ? (s == 3 ? ST_LAST : ST_PV0 + 2 + 2 * s + (i < 4 ? 0 : 1))
       : ST_EXP;
}
constexpr int free_step(int kind, int s, int i) { return kind == KPX ? ST_PRD : need_step(kind, s, i); }

struct Plan {
  int n;
  int kind[MAXLD], s[MAXLD], i[MAXLD], phys[MAXLD], at[MAXLD];
  int first[NSTEP + 2];
  int vm[NSTEP];
  bool ok;
};

constexpr Plan make_plan() {
  Plan p{};
  auto add = [&](int kind, int s, int i0, int cnt) { for (int c = 0; c < cnt; ++c) { p.kind[p.n] = kind; p.s[p.n] = s; p.i[p.n] = i0 + c; ++p.n; } };
  add(KX, 0, 0, 4); add(KWQ, 0, 0, 2); add(KK, 0, 0, 4); add(KX, 0, 4, 3); add(KWQ, 0, 2, 5);
  add(KWQ, 1, 0, 4); add(KK, 0, 4, 4); add(KWQ, 1, 4, 3);
  for (int s = 1; s < 3; ++s) { add(KK, s, 0, 4); add(KWQ, s + 1, 0, 4); add(KK, s, 4, 4); add(KWQ, s + 1, 4, 3); }
  add(KK, 3, 0, 4); add(KK, 3, 4, 4);
  add(KV, 0, 0, 4); add(KV, 1, 0, 4); add(KWO, 0, 0, 3); add(KPX, 0, 0, 8);
  add(KV, 0, 4, 4); add(KWO, 0, 3, 4); add(KV, 1, 4, 4);
  for (int s = 2; s < 4; ++s) { add(KV, s, 0, 4); add(KWO, s - 1, 0, 4); add(KV, s, 4, 4); add(KWO, s - 1, 4, 3); }
  add(KWO, 3, 0, 7);
  int owner[NSLOT] = {};
  for (int k = 0; k < NSLOT; ++k) owner[k] = -1;
  int nx = 0;
  p.ok = true;
  for (int t = -1; t < NSTEP; ++t) {
    for (int k = 0; k < NSLOT; ++k)
      if (owner[k] >= 0 && free_step(p.kind[owner[k]], p.s[owner[k]], p.i[owner[k]]) < t) owner[k] = -1;
    p.first[t + 1] = nx;
    for (int k = 0; k < NSLOT && nx < p.n; ++k)
      if (owner[k] < 0) { owner[k] = nx; p.phys[nx] = k; p.at[nx] = t; ++nx; }
  }
  p.first[NSTEP + 1] = nx;
  if (nx != p.n) p.ok = false;
  for (int e = 0; e < p.n; ++e) {
    const int nd = need_step(p.kind[e], p.s[e], p.i[e]);
    if (p.kind[e] == KPX ? p.at[e] > nd : p.at[e] >= nd) p.ok = false;
  }
  for (int t = 0; t < NSTEP; ++t) {
    int last = -1;
    for (int e = 0; e < p.n; ++e)
      if (p.kind[e] != KPX && need_step(p.kind[e], p.s[e], p.i[e]) <= t) last = e;
    int c = 0;
    for (int e = last + 1; e < p.first[t + 1]; ++e) if (p.kind[e] != KPX) ++c;
    p.vm[t] = c;
  }
  return p;
}
constexpr Plan PL = make_plan();
static_assert(PL.ok, "infeasible schedule");
constexpr int slot_of(int kind, int s, int i) {
  for (int e = 0; e < PL.n; ++e) if (PL.kind[e] == kind && PL.s[e] == s && PL.i[e] == i) return PL.phys[e] * 8192;
  return -1;
}

template <int I> struct IC { static constexpr int value = I; };
template <int A, int B, class F> DI void sfor(F&& f) { if constexpr (A < B) { f(IC<A>{}); sfor<A + 1, B>(f); } }

DI f32x16 mfma(s16x8 a, s16x8 b, f32x16 c) { return __builtin_amdgcn_mfma_f32_32x32x16_bf16(a, b, c, 0, 0, 0); }
DI u32 pk2(float lo, float hi) { f32x2 v = {lo, hi}; return __builtin_bit_cast(u32, __builtin_convertvector(v, bf16x2)); }
template <int S> DI u32x4 pack8(const f32x16& x) {
  u32x4 r = {pk2(x[8 * S], x[8 * S + 1]), pk2(x[8 * S + 2], x[8 * S + 3]), pk2(x[8 * S + 4], x[8 * S + 5]), pk2(x[8 * S + 6], x[8 * S + 7])};
  return r;
}
DI s16x8 lds_rd(u32 a) { return *(const LDSA s16x8*)(size_t)a; }
DI void lds_wr(u32 a, u32x4 v) { *(LDSA u32x4*)(size_t)a = v; }
DI s16x8 lds_tr(u32 a0, u32 a1) {
  const s16x4 lo = __builtin_amdgcn_ds_read_tr16_b64_v4i16((LDSA s16x4*)(size_t)a0);
  const s16x4 hi = __builtin_amdgcn_ds_read_tr16_b64_v4i16((LDSA s16x4*)(size_t)a1);
  return __builtin_shufflevector(lo, hi, 0, 1, 2, 3, 4, 5, 6, 7);
}
template <int VM> DI void step_barrier() { asm volatile("s_waitcnt vmcnt(%0) lgkmcnt(0)\n\ts_barrier" ::"i"(VM) : "memory"); }
DI float half_max(float v) {
  auto r = __builtin_amdgcn_permlane32_swap(__float_as_uint(v), __float_as_uint(v), false, false);
  return fmaxf(__uint_as_float(r[0]), __uint_as_float(r[1]));
}
DI float half_sum(float v) {
  auto r = __builtin_amdgcn_permlane32_swap(__float_as_uint(v), __float_as_uint(v), false, false);
  return __uint_as_float(r[0]) + __uint_as_float(r[1]);
}

struct Ctx {
  rsrc_t rx, rk, rv, rwq, rwo;
  u32 vx, vwq, vk, vv, vwo;
  u32 m0base;
  u32 sl[4];
  u32 kvs[4];
  u32 r7[7];
};

template <int OFF> DI void dma(u32 m0base, u32 voff, rsrc_t r, u32 soff) {
  asm volatile("s_add_u32 m0, %0, %1\n\ts_nop 0\n\tbuffer_load_dwordx4 %2, %3, %4 offen lds"
               ::"s"(m0base), "i"(OFF), "v"(voff), "s"(r), "s"(soff) : "scc");
}
template <int E> DI void issue(const Ctx& c) {
  constexpr int kind = PL.kind[E], s = PL.s[E], i = PL.i[E], off = PL.phys[E] * 8192;
  if constexpr (kind == KX) dma<off>(c.m0base, c.vx, c.rx, c.r7[i] * 128u);
  else if constexpr (kind == KWQ) dma<off>(c.m0base, c.vwq, c.rwq, c.sl[s] + c.r7[i] * 524288u);
  else if constexpr (kind == KK) dma<off>(c.m0base, c.vk, c.rk, c.kvs[s] ^ (u32)(i << 15));
  else if constexpr (kind == KV) dma<off>(c.m0base, c.vv, c.rv, c.kvs[s] ^ (u32)(i << 15));
  else if constexpr (kind == KWO) dma<off>(c.m0base, c.vwo, c.rwo, c.sl[s] * 448u + c.r7[i] * 128u);
}
template <int T, int PART, int NPART> DI void issue_part(const Ctx& c) {
  constexpr int a = PL.first[T + 1], n = PL.first[T + 2] - a;
  sfor<a + (n * PART + NPART - 1) / NPART, a + (n * (PART + 1) + NPART - 1) / NPART>([&](auto E) { issue<decltype(E)::value>(c); });
}

constexpr int SPIN_MAX = 1 << 20;

DI void role_attn(const Ctx& c, u32 lds, int w, int lane, int b, int h, char* ws, char* out, rsrc_t rf, u32 inbox) {
  const int r = lane & 31, hf = lane >> 5, wi = w & 1, wj = w >> 1;
  const u32 rowrd = lds + r * 128 + ((hf ^ ((r >> 1) & 7)) << 4);
  const int blk = (lane >> 4) & 1, q4 = (lane & 15) >> 2, p4 = lane & 3;
  const u32 trv = lds + (4 * hf + q4) * 128 + (((4 * wi + 2 * blk + (p4 >> 1)) ^ (((q4 >> 1) & 1) << 2)) << 4) + 8 * (p4 & 1);
  const u32 xch = lds + XCH + lane * 16;

  f32x16 S[2][2][2];
  sfor<0, 8>([&](auto I) { constexpr int i = decltype(I)::value; S[i >> 2][(i >> 1) & 1][i & 1] = f32x16{}; });
  s16x8 qf[2][4];
  u32 nonce = 0;

  sfor<0, 3>([&](auto T) {
    constexpr int t = decltype(T)::value;
    step_barrier<PL.vm[t]>();
    issue_part<t, 0, 1>(c);
    if constexpr (t == 0) {
      if (w == 0) {
        asm volatile("" : "+v"(inbox));
        u32 nn = __builtin_amdgcn_readfirstlane(inbox) + 1u;
        while (__builtin_amdgcn_ballot_w64(inbox == nn) != 0ull) ++nn;
        if (lane == 0) {
          asm volatile("buffer_store_dword %0, off, %1, %2 offset:128 sc1" ::"v"(nn), "s"(rf), "s"(h * 256) : "memory");
          *(LDSA u32*)(size_t)(lds + XCH) = nn;
        }
      }
    }
    if constexpr (t == 1) nonce = __builtin_amdgcn_readfirstlane(*(const LDSA u32*)(size_t)(lds + XCH));
  });

  float mx[2];
  sfor<3, ST_EXP>([&](auto T) {
    constexpr int t = decltype(T)::value, s = (t - 3) >> 1, half = (t - 3) & 1;
    step_barrier<PL.vm[t]>();
    if constexpr (half == 0) {
      sfor<0, 8>([&](auto I) { constexpr int i = decltype(I)::value; qf[i >> 2][i & 3] = lds_rd(xch + i * 1024); });
    }
    constexpr u32 s0 = slot_of(KK, s, 4 * half), s1 = slot_of(KK, s, 4 * half + 1), s2 = slot_of(KK, s, 4 * half + 2), s3 = slot_of(KK, s, 4 * half + 3);
    const u32 sb = w == 0 ? s0 : w == 1 ? s1 : w == 2 ? s2 : s3;
    sfor<0, 8>([&](auto I) {
      constexpr int i = decltype(I)::value, tt = i >> 2, ks = i & 3;
      const s16x8 kf = lds_rd(((rowrd ^ (u32)(ks << 5)) + sb) + tt * 4096);
      issue_part<t, i, 8>(c);
      S[half][tt][0] = mfma(kf, qf[0][ks], S[half][tt][0]);
      S[half][tt][1] = mfma(kf, qf[1][ks], S[half][tt][1]);
    });
    if constexpr (t == ST_EXP - 1) {
      sfor<0, 2>([&](auto Q) {
        constexpr int qb = decltype(Q)::value;
        float m = S[0][0][qb][0];
        sfor<0, 4>([&](auto I) { constexpr int i = decltype(I)::value;
          for (int e = 0; e < 16; ++e) m = fmaxf(m, S[i >> 1][i & 1][qb][e]); });
        mx[qb] = half_max(m);
      });
      *(LDSA float*)(size_t)(lds + XCH + (lane * 4 + w) * 4) = hf ? mx[1] : mx[0];
    }
  });

  step_barrier<PL.vm[ST_EXP]>();
  issue_part<ST_EXP, 0, 1>(c);
  {
    constexpr float CS = 0.0625f * 1.4426950408889634f;
    float sum[2];
    sfor<0, 2>([&](auto Q) {
      constexpr int qb = decltype(Q)::value;
      const f32x4 mm = *(const LDSA f32x4*)(size_t)(lds + XCH + (qb * 32 + r) * 16);
      const float nm = -CS * fmaxf(fmaxf(mm[0], mm[1]), fmaxf(mm[2], mm[3]));
      float acc = 0.f;
      sfor<0, 4>([&](auto I) {
        constexpr int i = decltype(I)::value, half = i >> 1, tt = i & 1;
        f32x16 e;
        for (int k = 0; k < 16; ++k) { e[k] = __builtin_amdgcn_exp2f(__builtin_fmaf(S[half][tt][qb][k], CS, nm)); acc += e[k]; }
        constexpr u32 pa = slot_of(KPX, 0, qb * 4 + 2 * half), pb = slot_of(KPX, 0, qb * 4 + 2 * half + 1);
        const u32 dst = lds + (wj ? pb : pa) + wi * 4096 + tt * 2048 + lane * 16;
        lds_wr(dst, pack8<0>(e));
        lds_wr(dst + 1024, pack8<1>(e));
      });
      sum[qb] = half_sum(acc);
    });
    *(LDSA float*)(size_t)(lds + XCH + 1024 + (lane * 4 + w) * 4) = hf ? sum[1] : sum[0];
  }

  s16x8 pf[32];
  float linv;
  step_barrier<PL.vm[ST_PRD]>();
  issue_part<ST_PRD, 0, 1>(c);
  sfor<0, 4>([&](auto G) {
    constexpr int g = decltype(G)::value;
    constexpr u32 pa = slot_of(KPX, 0, g), pb = slot_of(KPX, 0, 4 + g);
    const u32 src = lds + (wj ? pb : pa) + lane * 16;
    sfor<0, 8>([&](auto I) { constexpr int i = decltype(I)::value; pf[g * 8 + i] = lds_rd(src + i * 1024); });
  });
  {
    const f32x4 ss = *(const LDSA f32x4*)(size_t)(lds + XCH + 1024 + (wj * 32 + r) * 16);
    linv = 1.0f / ((ss[0] + ss[1]) + (ss[2] + ss[3]));
  }

  f32x16 O;
  sfor<ST_PV0, ST_LAST>([&](auto T) {
    constexpr int t = decltype(T)::value, s = (t - ST_PV0) >> 1, half = (t - ST_PV0) & 1;
    step_barrier<PL.vm[t]>();
    if constexpr (half == 0) O = f32x16{};
    sfor<0, 16>([&](auto I) {
      constexpr int i = decltype(I)::value, u = 4 * half + (i >> 2), ks = i & 3;
      constexpr u32 sb = slot_of(KV, s, u) + ks * 2048;
      const s16x8 vf = lds_tr(trv + sb, trv + sb + 1024);
      if constexpr ((i & 1) == 0) issue_part<t, (i >> 1), 8>(c);
      O = mfma(vf, pf[4 * u + ks], O);
    });
    if constexpr (half == 1) {
      for (int k = 0; k < 16; ++k) O[k] *= linv;
      lds_wr(xch + (wj * 4 + wi * 2) * 1024, pack8<0>(O));
      lds_wr(xch + (wj * 4 + wi * 2 + 1) * 1024, pack8<1>(O));
    }
  });

  step_barrier<PL.vm[ST_LAST]>();
  for (int it = 0; it < SPIN_MAX; ++it) {
    const u32 v = __builtin_amdgcn_raw_buffer_load_b32(rf, r * 4, h * 256, 16);
    if (__builtin_amdgcn_ballot_w64(v != nonce) == 0ull) break;
    asm volatile("" ::: "memory");
  }
  asm volatile("s_barrier" ::: "memory");
  const int tix = w * 64 + lane;
  if (tix < 224) {
    const rsrc_t rp = __builtin_amdgcn_make_buffer_rsrc(ws + (size_t)(b * 16 + h) * PART_BYTES, 0, PART_BYTES, 0x00020000);
    u32x4 pv[16];
    for (int p = 0; p < 16; ++p) pv[p] = __builtin_amdgcn_raw_buffer_load_b128(rp, tix * 16, p * 3584, 16);
    float a[8];
    for (int k = 0; k < 8; ++k) a[k] = 0.f;
    for (int p = 0; p < 16; ++p)
      for (int k = 0; k < 4; ++k) { a[2 * k] += __uint_as_float(pv[p][k] << 16); a[2 * k + 1] += __uint_as_float(pv[p][k] & 0xffff0000u); }
    const u32x4 o = {pk2(a[0], a[1]), pk2(a[2], a[3]), pk2(a[4], a[5]), pk2(a[6], a[7])};
    *(u32x4*)(out + (size_t)(b * 64 + 4 * h + (tix & 3)) * 896 + (tix >> 2) * 16) = o;
  }
}

DI void role_proj(const Ctx& c, u32 lds, int w, int lane, int b, int h, char* ws, rsrc_t rf) {
  const int r = lane & 31, hf = lane >> 5, wi = w & 1, wj = w >> 1;
  const u32 rowrd = lds + (wj * 32 + r) * 128 + ((hf ^ ((r >> 1) & 7)) << 4);
  const int blk = (lane >> 4) & 1, q4 = (lane & 15) >> 2, p4 = lane & 3;
  const u32 trc = (((4 * wi + 2 * blk + (p4 & 1)) ^ (((q4 >> 1) & 1) << 2)) << 4) + 8 * (p4 >> 1);
  const u32 trq = lds + (8 * hf + q4) * 128 + trc;
  const u32 tro = lds + (4 * hf + q4) * 128 + trc;
  const u32 xch = lds + XCH + lane * 16;

  {
    s16x8 xf[28];
    f32x16 Q;
    sfor<0, 9>([&](auto T) {
      constexpr int t = decltype(T)::value;
      step_barrier<PL.vm[t]>();
      if constexpr (t <= 1) {
        sfor<(t == 0 ? 0 : 16), (t == 0 ? 16 : 28)>([&](auto I) {
          constexpr int i = decltype(I)::value;
          constexpr u32 sb = slot_of(KX, 0, i >> 2);
          xf[i] = lds_rd((rowrd ^ (u32)((i & 3) << 5)) + sb);
        });
      }
      if constexpr (t == 0) issue_part<t, 0, 1>(c);
      else {
        constexpr int s = (t - 1) >> 1, half = (t - 1) & 1, n = half ? 12 : 16;
        if constexpr (half == 0) Q = f32x16{};
        sfor<0, n>([&](auto I) {
          constexpr int i = decltype(I)::value, u = 4 * half + (i >> 2), ks = i & 3;
          constexpr u32 sb = slot_of(KWQ, s, u) + ks * 2048;
          const s16x8 wf = lds_tr(trq + sb, trq + sb + 512);
          if constexpr ((i & 1) == 0) issue_part<t, (i >> 1), n / 2>(c);
          Q = mfma(wf, xf[4 * u + ks], Q);
        });
        if constexpr (half == 1) {
          lds_wr(xch + (wj * 4 + wi * 2) * 1024, pack8<0>(Q));
          lds_wr(xch + (wj * 4 + wi * 2 + 1) * 1024, pack8<1>(Q));
        }
      }
    });
  }

  u32 req;
  const u32 creq = (8 * wj + (lane & 7)) * 256;
  sfor<9, ST_PV0 + 2>([&](auto T) {
    constexpr int t = decltype(T)::value;
    step_barrier<PL.vm[t]>();
    if constexpr (t == ST_EXP) asm volatile("buffer_load_dword %0, %1, %2, 0 offen offset:128 sc1" : "=v"(req) : "v"(creq), "s"(rf));
    issue_part<t, 0, 1>(c);
  });

  f32x16 acc[7];
  sfor<0, 7>([&](auto I) { acc[decltype(I)::value] = f32x16{}; });
  s16x8 of[4];
  const rsrc_t rp = __builtin_amdgcn_make_buffer_rsrc(ws + (size_t)b * 16 * PART_BYTES, 0, 16 * PART_BYTES, 0x00020000);
  const u32 vst = (8 * wj + (r >> 2)) * PART_BYTES + hf * 64 + (r & 3) * 16 + wi * 256;
  sfor<ST_PV0 + 2, NSTEP>([&](auto T) {
    constexpr int t = decltype(T)::value, s = (t - ST_PV0 - 2) >> 1, half = (t - ST_PV0 - 2) & 1;
    constexpr int u0 = 4 * half, n = t == ST_LAST ? 28 : half ? 12 : 16;
    step_barrier<PL.vm[t]>();
    if constexpr (half == 0) {
      sfor<0, 4>([&](auto I) { constexpr int i = decltype(I)::value; of[i] = lds_rd(xch + (wj * 4 + i) * 1024); });
    }
    sfor<0, n>([&](auto I) {
      constexpr int i = decltype(I)::value, u = u0 + (i >> 2), ks = i & 3;
      constexpr u32 sb = slot_of(KWO, s, u) + ks * 2048;
      const s16x8 wf = lds_tr(tro + sb, tro + sb + 1024);
      if constexpr ((i & 1) == 0) issue_part<t, (i >> 1), n / 2>(c);
      acc[u] = mfma(wf, of[ks], acc[u]);
      if constexpr (t == ST_LAST && ks == 3) {
        const u32 so = h * 3584 + c.r7[u] * 512;
        __builtin_amdgcn_raw_buffer_store_b128(pack8<0>(acc[u]), rp, vst, so, 16);
        __builtin_amdgcn_raw_buffer_store_b128(pack8<1>(acc[u]), rp, vst + 128, so, 16);
      }
    });
  });

  asm volatile("s_waitcnt vmcnt(0)" : "+v"(req)::"memory");
  const u32 cin = creq + (h * 2 + wi) * 4;
  if (lane < 8) __builtin_amdgcn_raw_buffer_store_b32(req, rf, cin, 0, 16);
  asm volatile("s_barrier" ::: "memory");
  const u32 now = __builtin_amdgcn_raw_buffer_load_b32(rf, creq + 128, 0, 16);
  if (lane < 8 && now != req) __builtin_amdgcn_raw_buffer_store_b32(now, rf, cin, 0, 16);
}

__global__ __launch_bounds__(512, 2) void gqa_block_kernel(const char* __restrict__ x, const char* __restrict__ k, const char* __restrict__ v,
                                                           const char* __restrict__ wq, const char* __restrict__ wo, char* out, char* ws) {
  __shared__ __attribute__((aligned(16))) char smem[LDS_BYTES];
  const u32 lds = (u32)(size_t)(LDSA char*)smem;
  const int bid = blockIdx.x, kvh = bid & 7, b = bid >> 4, hh = (bid >> 3) & 1, h = kvh * 2 + hh;
  const int wid = __builtin_amdgcn_readfirstlane(threadIdx.x >> 6), lane = threadIdx.x & 63;

  Ctx c;
  c.rx = __builtin_amdgcn_make_buffer_rsrc((void*)(x + (size_t)b * 57344), 0, 57344, 0x00020000);
  c.rk = __builtin_amdgcn_make_buffer_rsrc((void*)(k + (size_t)(b * 8 + kvh) * 262144), 0, 262144, 0x00020000);
  c.rv = __builtin_amdgcn_make_buffer_rsrc((void*)(v + (size_t)(b * 8 + kvh) * 262144), 0, 262144, 0x00020000);
  c.rwq = __builtin_amdgcn_make_buffer_rsrc((void*)(wq + h * 512), 0, 3670016 - h * 512, 0x00020000);
  c.rwo = __builtin_amdgcn_make_buffer_rsrc((void*)(wo + (size_t)h * 229376), 0, 229376, 0x00020000);
  const u32 row = wid * 8 + (lane >> 3);
  const u32 crow = ((lane & 7) ^ ((row >> 1) & 7)) << 4;
  const u32 ctr = ((lane & 7) ^ (((row >> 1) & 1) << 2)) << 4;
  c.vx = row * 896 + crow; c.vk = row * 512 + crow;
  c.vwq = row * 8192 + ctr; c.vv = row * 512 + ctr; c.vwo = row * 896 + ctr;
  c.m0base = lds + wid * 1024;
  for (int s = 0; s < 4; ++s) { c.sl[s] = ((s + b) & 3) * 128; c.kvs[s] = c.sl[s] | (hh << 17); }
  const int wr = (b >> 2) * 2 + hh * 3;
  for (int i = 0; i < 7; ++i) { const int t = i + wr; c.r7[i] = t >= 14 ? t - 14 : t >= 7 ? t - 7 : t; }

  const rsrc_t rf = __builtin_amdgcn_make_buffer_rsrc(ws + WS_FLAGS + b * 4096, 0, 4096, 0x00020000);
  u32 inbox = 0;
  if (wid == 0) asm volatile("buffer_load_dword %0, %1, %2, %3 offen sc1" : "=v"(inbox) : "v"((lane & 31) * 4), "s"(rf), "s"(h * 256));
  issue_part<-1, 0, 1>(c);
  if (wid < 4) role_attn(c, lds, wid, lane, b, h, ws, out, rf, inbox);
  else role_proj(c, lds, wid - 4, lane, b, h, ws, rf);
}

}

extern "C" void kernel_launch(void* const* d_in, const int* in_sizes, int n_in, void* d_out, int out_size, void* d_ws, size_t ws_size,
                              hipStream_t stream) {
  gqa_block_kernel<<<256, 512, 0, stream>>>((const char*)d_in[0], (const char*)d_in[1], (const char*)d_in[2], (const char*)d_in[3],
                                            (const char*)d_in[4], (char*)d_out, (char*)d_ws);
}
```

```cpp
#define STAMPS 1
#include <hip/hip_runtime.h>

namespace {

typedef unsigned int u32;
typedef unsigned long long u64;
typedef short s16x4 __attribute__((ext_vector_type(4)));
typedef short s16x8 __attribute__((ext_vector_type(8)));
typedef float f32x16 __attribute__((ext_vector_type(16)));
typedef float f32x2 __attribute__((ext_vector_type(2)));
typedef float f32x4 __attribute__((ext_vector_type(4)));
typedef __bf16 bf16x2 __attribute__((ext_vector_type(2)));
typedef u32 u32x4 __attribute__((ext_vector_type(4)));
#define LDSA __attribute__((address_space(3)))
#define DI __device__ __forceinline__
typedef __amdgpu_buffer_rsrc_t rsrc_t;

constexpr int NSLOT = 19;
constexpr int XCH = NSLOT * 8192;
constexpr int LDS_BYTES = XCH + 8192;
constexpr u32 PART_BYTES = 57344;
constexpr u32 WS_FLAGS = 16u * 16u * PART_BYTES;

enum { KX = 0, KWQ = 1, KK = 2, KV = 3, KWO = 4, KPX = 5 };
constexpr int NSTEP = 22;
constexpr int ST_EXP = 11, ST_PRD = 12, ST_PV0 = 13, ST_LAST = 21;
constexpr int MAXLD = 136;

constexpr int need_step(int kind, int s, int i) {
  return kind == KX ? (i < 4 ? 0 : 1)
       : kind == KWQ ? 1 + 2 * s + (i < 4 ? 0 : 1)
       : kind == KK ? 3 + 2 * s + (i < 4 ? 0 : 1)
       : kind == KV ? ST_PV0 + 2 * s + (i < 4 ? 0 : 1)
       : kind == KWO ? (s == 3 ? ST_LAST : ST_PV0 + 2 + 2 * s + (i < 4 ? 0 : 1))
       : ST_EXP;
}
constexpr int free_step(int kind, int s, int i) { return kind == KPX ? ST_PRD : need_step(kind, s, i); }

struct Plan {
  int n;
  int kind[MAXLD], s[MAXLD], i[MAXLD], phys[MAXLD], at[MAXLD];
  int first[NSTEP + 2];
  int vm[NSTEP];
  bool ok;
};

constexpr Plan make_plan() {
  Plan p{};
  auto add = [&](int kind, int s, int i0, int cnt) { for (int c = 0; c < cnt; ++c) { p.kind[p.n] = kind; p.s[p.n] = s; p.i[p.n] = i0 + c; ++p.n; } };
  add(KX, 0, 0, 4); add(KWQ, 0, 0, 2); add(KK, 0, 0, 4); add(KX, 0, 4, 3); add(KWQ, 0, 2, 5);
  add(KWQ, 1, 0, 4); add(KK, 0, 4, 4); add(KWQ, 1, 4, 3);
  for (int s = 1; s < 3; ++s) { add(KK, s, 0, 4); add(KWQ, s + 1, 0, 4); add(KK, s, 4, 4); add(KWQ, s + 1, 4, 3); }
  add(KK, 3, 0, 4); add(KK, 3, 4, 4);
  add(KV, 0, 0, 4); add(KV, 1, 0, 4); add(KWO, 0, 0, 3); add(KPX, 0, 0, 8);
  add(KV, 0, 4, 4); add(KWO, 0, 3, 4); add(KV, 1, 4, 4);
  for (int s = 2; s < 4; ++s) { add(KV, s, 0, 4); add(KWO, s - 1, 0, 4); add(KV, s, 4, 4); add(KWO, s - 1, 4, 3); }
  add(KWO, 3, 0, 7);
  int owner[NSLOT] = {};
  for (int k = 0; k < NSLOT; ++k) owner[k] = -1;
  int nx = 0;
  p.ok = true;
  for (int t = -1; t < NSTEP; ++t) {
    for (int k = 0; k < NSLOT; ++k)
      if (owner[k] >= 0 && free_step(p.kind[owner[k]], p.s[owner[k]], p.i[owner[k]]) < t) owner[k] = -1;
    p.first[t + 1] = nx;
    for (int k = 0; k < NSLOT && nx < p.n; ++k)
      if (owner[k] < 0) { owner[k] = nx; p.phys[nx] = k; p.at[nx] = t; ++nx; }
  }
  p.first[NSTEP + 1] = nx;
  if (nx != p.n) p.ok = false;
  for (int e = 0; e < p.n; ++e) {
    const int nd = need_step(p.kind[e], p.s[e], p.i[e]);
    if (p.kind[e] == KPX ? p.at[e] > nd : p.at[e] >= nd) p.ok = false;
  }
  for (int t = 0; t < NSTEP; ++t) {
    int last = -1;
    for (int e = 0; e < p.n; ++e)
      if (p.kind[e] != KPX && need_step(p.kind[e], p.s[e], p.i[e]) <= t) last = e;
    int c = 0;
    for (int e = last + 1; e < p.first[t + 1]; ++e) if (p.kind[e] != KPX) ++c;
    p.vm[t] = c;
  }
  return p;
}
constexpr Plan PL = make_plan();
static_assert(PL.ok, "infeasible schedule");
constexpr int slot_of(int kind, int s, int i) {
  for (int e = 0; e < PL.n; ++e) if (PL.kind[e] == kind && PL.s[e] == s && PL.i[e] == i) return PL.phys[e] * 8192;
  return -1;
}

template <int I> struct IC { static constexpr int value = I; };
template <int A, int B, class F> DI void sfor(F&& f) { if constexpr (A < B) { f(IC<A>{}); sfor<A + 1, B>(f); } }

DI f32x16 mfma(s16x8 a, s16x8 b, f32x16 c) { return __builtin_amdgcn_mfma_f32_32x32x16_bf16(a, b, c, 0, 0, 0); }
DI u32 pk2(float lo, float hi) { f32x2 v = {lo, hi}; return __builtin_bit_cast(u32, __builtin_convertvector(v, bf16x2)); }
template <int S> DI u32x4 pack8(const f32x16& x) {
  u32x4 r = {pk2(x[8 * S], x[8 * S + 1]), pk2(x[8 * S + 2], x[8 * S + 3]), pk2(x[8 * S + 4], x[8 * S + 5]), pk2(x[8 * S + 6], x[8 * S + 7])};
  return r;
}
DI s16x8 lds_rd(u32 a) { return *(const LDSA s16x8*)(size_t)a; }
DI void lds_wr(u32 a, u32x4 v) { *(LDSA u32x4*)(size_t)a = v; }
DI s16x8 lds_tr(u32 a0, u32 a1) {
  const s16x4 lo = __builtin_amdgcn_ds_read_tr16_b64_v4i16((LDSA s16x4*)(size_t)a0);
  const s16x4 hi = __builtin_amdgcn_ds_read_tr16_b64_v4i16((LDSA s16x4*)(size_t)a1);
  return __builtin_shufflevector(lo, hi, 0, 1, 2, 3, 4, 5, 6, 7);
}
constexpr int NSTAMP = 64;
constexpr size_t STAMP_OFF = 32u << 20;
#define STAMP(idx) do { u64 t_; asm volatile("s_memrealtime %0\n\ts_waitcnt lgkmcnt(0)" : "=s"(t_)::"memory"); asm volatile("v_writelane_b32 %0, %1, %2" : "+v"(stv) : "s"((u32)t_), "i"(idx)); } while (0)
#define STAMP_OUT(role) do { if (w == 0) ((u32*)(ws + STAMP_OFF))[(blockIdx.x * 2 + (role)) * 64 + lane] = stv; } while (0)
template <int VM> DI void step_barrier_() { asm volatile("s_waitcnt vmcnt(%0) lgkmcnt(0)\n\ts_barrier" ::"i"(VM) : "memory"); }
#define STEP_BARRIER(t) do { STAMP(2 * (t) + 1); step_barrier_<PL.vm[t]>(); STAMP(2 * (t) + 2); } while (0)
DI float half_max(float v) {
  auto r = __builtin_amdgcn_permlane32_swap(__float_as_uint(v), __float_as_uint(v), false, false);
  return fmaxf(__uint_as_float(r[0]), __uint_as_float(r[1]));
}
DI float half_sum(float v) {
  auto r = __builtin_amdgcn_permlane32_swap(__float_as_uint(v), __float_as_uint(v), false, false);
  return __uint_as_float(r[0]) + __uint_as_float(r[1]);
}

struct Ctx {
  rsrc_t rx, rk, rv, rwq, rwo;
  u32 vx, vwq, vk, vv, vwo;
  u32 m0base;
  u32 sl[4];
  u32 kvs[4];
  u32 r7[7];
};

template <int OFF> DI void dma(u32 m0base, u32 voff, rsrc_t r, u32 soff) {
  asm volatile("s_add_u32 m0, %0, %1\n\ts_nop 0\n\tbuffer_load_dwordx4 %2, %3, %4 offen lds"
               ::"s"(m0base), "i"(OFF), "v"(voff), "s"(r), "s"(soff) : "scc");
}
template <int E> DI void issue(const Ctx& c) {
  constexpr int kind = PL.kind[E], s = PL.s[E], i = PL.i[E], off = PL.phys[E] * 8192;
  if constexpr (kind == KX) dma<off>(c.m0base, c.vx, c.rx, c.r7[i] * 128u);
  else if constexpr (kind == KWQ) dma<off>(c.m0base, c.vwq, c.rwq, c.sl[s] + c.r7[i] * 524288u);
  else if constexpr (kind == KK) dma<off>(c.m0base, c.vk, c.rk, c.kvs[s] ^ (u32)(i << 15));
  else if constexpr (kind == KV) dma<off>(c.m0base, c.vv, c.rv, c.kvs[s] ^ (u32)(i << 15));
  else if constexpr (kind == KWO) dma<off>(c.m0base, c.vwo, c.rwo, c.sl[s] * 448u + c.r7[i] * 128u);
}
template <int T, int PART, int NPART> DI void issue_part(const Ctx& c) {
  constexpr int a = PL.first[T + 1], n = PL.first[T + 2] - a;
  sfor<a + (n * PART + NPART - 1) / NPART, a + (n * (PART + 1) + NPART - 1) / NPART>([&](auto E) { issue<decltype(E)::value>(c); });
}

constexpr int SPIN_MAX = 1 << 20;

DI void role_attn(const Ctx& c, u32 lds, int w, int lane, int b, int h, char* ws, char* out, rsrc_t rf, u32 inbox, u32 stv) {
  const int r = lane & 31, hf = lane >> 5, wi = w & 1, wj = w >> 1;
  const u32 rowrd = lds + r * 128 + ((hf ^ ((r >> 1) & 7)) << 4);
  const int blk = (lane >> 4) & 1, q4 = (lane & 15) >> 2, p4 = lane & 3;
  const u32 trv = lds + (4 * hf + q4) * 128 + (((4 * wi + 2 * blk + (p4 >> 1)) ^ (((q4 >> 1) & 1) << 2)) << 4) + 8 * (p4 & 1);
  const u32 xch = lds + XCH + lane * 16;

  f32x16 S[2][2][2];
  sfor<0, 8>([&](auto I) { constexpr int i = decltype(I)::value; S[i >> 2][(i >> 1) & 1][i & 1] = f32x16{}; });
  s16x8 qf[2][4];
  u32 nonce = 0;

  sfor<0, 3>([&](auto T) {
    constexpr int t = decltype(T)::value;
    STEP_BARRIER(t);
    issue_part<t, 0, 1>(c);
    if constexpr (t == 0) {
      if (w == 0) {
        asm volatile("" : "+v"(inbox));
        u32 nn = __builtin_amdgcn_readfirstlane(inbox) + 1u;
        while (__builtin_amdgcn_ballot_w64(inbox == nn) != 0ull) ++nn;
        if (lane == 0) {
          asm volatile("buffer_store_dword %0, off, %1, %2 offset:128 sc1" ::"v"(nn), "s"(rf), "s"(h * 256) : "memory");
          *(LDSA u32*)(size_t)(lds + XCH) = nn;
        }
      }
    }
    if constexpr (t == 1) nonce = __builtin_amdgcn_readfirstlane(*(const LDSA u32*)(size_t)(lds + XCH));
  });

  float mx[2];
  sfor<3, ST_EXP>([&](auto T) {
    constexpr int t = decltype(T)::value, s = (t - 3) >> 1, half = (t - 3) & 1;
    STEP_BARRIER(t);
    if constexpr (half == 0) {
      sfor<0, 8>([&](auto I) { constexpr int i = decltype(I)::value; qf[i >> 2][i & 3] = lds_rd(xch + i * 1024); });
    }
    constexpr u32 s0 = slot_of(KK, s, 4 * half), s1 = slot_of(KK, s, 4 * half + 1), s2 = slot_of(KK, s, 4 * half + 2), s3 = slot_of(KK, s, 4 * half + 3);
    const u32 sb = w == 0 ? s0 : w == 1 ? s1 : w == 2 ? s2 : s3;
    sfor<0, 8>([&](auto I) {
      constexpr int i = decltype(I)::value, tt = i >> 2, ks = i & 3;
      const s16x8 kf = lds_rd(((rowrd ^ (u32)(ks << 5)) + sb) + tt * 4096);
      issue_part<t, i, 8>(c);
      S[half][tt][0] = mfma(kf, qf[0][ks], S[half][tt][0]);
      S[half][tt][1] = mfma(kf, qf[1][ks], S[half][tt][1]);
    });
    if constexpr (t == ST_EXP - 1) {
      sfor<0, 2>([&](auto Q) {
        constexpr int qb = decltype(Q)::value;
        float m = S[0][0][qb][0];
        sfor<0, 4>([&](auto I) { constexpr int i = decltype(I)::value;
          for (int e = 0; e < 16; ++e) m = fmaxf(m, S[i >> 1][i & 1][qb][e]); });
        mx[qb] = half_max(m);
      });
      *(LDSA float*)(size_t)(lds + XCH + (lane * 4 + w) * 4) = hf ? mx[1] : mx[0];
    }
  });

  STEP_BARRIER(ST_EXP);
  issue_part<ST_EXP, 0, 1>(c);
  {
    constexpr float CS = 0.0625f * 1.4426950408889634f;
    float sum[2];
    sfor<0, 2>([&](auto Q) {
      constexpr int qb = decltype(Q)::value;
      const f32x4 mm = *(const LDSA f32x4*)(size_t)(lds + XCH + (qb * 32 + r) * 16);
      const float nm = -CS * fmaxf(fmaxf(mm[0], mm[1]), fmaxf(mm[2], mm[3]));
      float acc = 0.f;
      sfor<0, 4>([&](auto I) {
        constexpr int i = decltype(I)::value, half = i >> 1, tt = i & 1;
        f32x16 e;
        for (int k = 0; k < 16; ++k) { e[k] = __builtin_amdgcn_exp2f(__builtin_fmaf(S[half][tt][qb][k], CS, nm)); acc += e[k]; }
        constexpr u32 pa = slot_of(KPX, 0, qb * 4 + 2 * half), pb = slot_of(KPX, 0, qb * 4 + 2 * half + 1);
        const u32 dst = lds + (wj ? pb : pa) + wi * 4096 + tt * 2048 + lane * 16;
        lds_wr(dst, pack8<0>(e));
        lds_wr(dst + 1024, pack8<1>(e));
      });
      sum[qb] = half_sum(acc);
    });
    *(LDSA float*)(size_t)(lds + XCH + 1024 + (lane * 4 + w) * 4) = hf ? sum[1] : sum[0];
  }

  s16x8 pf[32];
  float linv;
  STEP_BARRIER(ST_PRD);
  issue_part<ST_PRD, 0, 1>(c);
  sfor<0, 4>([&](auto G) {
    constexpr int g = decltype(G)::value;
    constexpr u32 pa = slot_of(KPX, 0, g), pb = slot_of(KPX, 0, 4 + g);
    const u32 src = lds + (wj ? pb : pa) + lane * 16;
    sfor<0, 8>([&](auto I) { constexpr int i = decltype(I)::value; pf[g * 8 + i] = lds_rd(src + i * 1024); });
  });
  {
    const f32x4 ss = *(const LDSA f32x4*)(size_t)(lds + XCH + 1024 + (wj * 32 + r) * 16);
    linv = 1.0f / ((ss[0] + ss[1]) + (ss[2] + ss[3]));
  }

  f32x16 O;
  sfor<ST_PV0, ST_LAST>([&](auto T) {
    constexpr int t = decltype(T)::value, s = (t - ST_PV0) >> 1, half = (t - ST_PV0) & 1;
    STEP_BARRIER(t);
    if constexpr (half == 0) O = f32x16{};
    sfor<0, 16>([&](auto I) {
      constexpr int i = decltype(I)::value, u = 4 * half + (i >> 2), ks = i & 3;
      constexpr u32 sb = slot_of(KV, s, u) + ks * 2048;
      const s16x8 vf = lds_tr(trv + sb, trv + sb + 1024);
      if constexpr ((i & 1) == 0) issue_part<t, (i >> 1), 8>(c);
      O = mfma(vf, pf[4 * u + ks], O);
    });
    if constexpr (half == 1) {
      for (int k = 0; k < 16; ++k) O[k] *= linv;
      lds_wr(xch + (wj * 4 + wi * 2) * 1024, pack8<0>(O));
      lds_wr(xch + (wj * 4 + wi * 2 + 1) * 1024, pack8<1>(O));
    }
  });

  STEP_BARRIER(ST_LAST);
  for (int it = 0; it < SPIN_MAX; ++it) {
    const u32 v = __builtin_amdgcn_raw_buffer_load_b32(rf, r * 4, h * 256, 16);
    if (__builtin_amdgcn_ballot_w64(v != nonce) == 0ull) break;
    asm volatile("" ::: "memory");
  }
  STAMP(2 * NSTEP + 1);
  asm volatile("s_barrier" ::: "memory");
  const int tix = w * 64 + lane;
  if (tix < 224) {
    const rsrc_t rp = __builtin_amdgcn_make_buffer_rsrc(ws + (size_t)(b * 16 + h) * PART_BYTES, 0, PART_BYTES, 0x00020000);
    u32x4 pv[16];
    for (int p = 0; p < 16; ++p) pv[p] = __builtin_amdgcn_raw_buffer_load_b128(rp, tix * 16, p * 3584, 16);
    float a[8];
    for (int k = 0; k < 8; ++k) a[k] = 0.f;
    for (int p = 0; p < 16; ++p)
      for (int k = 0; k < 4; ++k) { a[2 * k] += __uint_as_float(pv[p][k] << 16); a[2 * k + 1] += __uint_as_float(pv[p][k] & 0xffff0000u); }
    const u32x4 o = {pk2(a[0], a[1]), pk2(a[2], a[3]), pk2(a[4], a[5]), pk2(a[6], a[7])};
    STAMP(2 * NSTEP + 2);
    *(u32x4*)(out + (size_t)(b * 64 + 4 * h + (tix & 3)) * 896 + (tix >> 2) * 16) = o;
  }
  asm volatile("s_waitcnt vmcnt(0)" ::: "memory");
  STAMP(2 * NSTEP + 3);
  STAMP_OUT(0);
}

DI void role_proj(const Ctx& c, u32 lds, int w, int lane, int b, int h, char* ws, rsrc_t rf, u32 stv) {
  const int r = lane & 31, hf = lane >> 5, wi = w & 1, wj = w >> 1;
  const u32 rowrd = lds + (wj * 32 + r) * 128 + ((hf ^ ((r >> 1) & 7)) << 4);
  const int blk = (lane >> 4) & 1, q4 = (lane & 15) >> 2, p4 = lane & 3;
  const u32 trc = (((4 * wi + 2 * blk + (p4 & 1)) ^ (((q4 >> 1) & 1) << 2)) << 4) + 8 * (p4 >> 1);
  const u32 trq = lds + (8 * hf + q4) * 128 + trc;
  const u32 tro = lds + (4 * hf + q4) * 128 + trc;
  const u32 xch = lds + XCH + lane * 16;

  {
    s16x8 xf[28];
    f32x16 Q;
    sfor<0, 9>([&](auto T) {
      constexpr int t = decltype(T)::value;
      STEP_BARRIER(t);
      if constexpr (t <= 1) {
        sfor<(t == 0 ? 0 : 16), (t == 0 ? 16 : 28)>([&](auto I) {
          constexpr int i = decltype(I)::value;
          constexpr u32 sb = slot_of(KX, 0, i >> 2);
          xf[i] = lds_rd((rowrd ^ (u32)((i & 3) << 5)) + sb);
        });
      }
      if constexpr (t == 0) issue_part<t, 0, 1>(c);
      else {
        constexpr int s = (t - 1) >> 1, half = (t - 1) & 1, n = half ? 12 : 16;
        if constexpr (half == 0) Q = f32x16{};
        sfor<0, n>([&](auto I) {
          constexpr int i = decltype(I)::value, u = 4 * half + (i >> 2), ks = i & 3;
          constexpr u32 sb = slot_of(KWQ, s, u) + ks * 2048;
          const s16x8 wf = lds_tr(trq + sb, trq + sb + 512);
          if constexpr ((i & 1) == 0) issue_part<t, (i >> 1), n / 2>(c);
          Q = mfma(wf, xf[4 * u + ks], Q);
        });
        if constexpr (half == 1) {
          lds_wr(xch + (wj * 4 + wi * 2) * 1024, pack8<0>(Q));
          lds_wr(xch + (wj * 4 + wi * 2 + 1) * 1024, pack8<1>(Q));
        }
      }
    });
  }

  u32 req;
  const u32 creq = (8 * wj + (lane & 7)) * 256;
  sfor<9, ST_PV0 + 2>([&](auto T) {
    constexpr int t = decltype(T)::value;
    STEP_BARRIER(t);
    if constexpr (t == ST_EXP) asm volatile("buffer_load_dword %0, %1, %2, 0 offen offset:128 sc1" : "=v"(req) : "v"(creq), "s"(rf));
    issue_part<t, 0, 1>(c);
  });

  f32x16 acc[7];
  sfor<0, 7>([&](auto I) { acc[decltype(I)::value] = f32x16{}; });
  s16x8 of[4];
  const rsrc_t rp = __builtin_amdgcn_make_buffer_rsrc(ws + (size_t)b * 16 * PART_BYTES, 0, 16 * PART_BYTES, 0x00020000);
  const u32 vst = (8 * wj + (r >> 2)) * PART_BYTES + hf * 64 + (r & 3) * 16 + wi * 256;
  sfor<ST_PV0 + 2, NSTEP>([&](auto T) {
    constexpr int t = decltype(T)::value, s = (t - ST_PV0 - 2) >> 1, half = (t - ST_PV0 - 2) & 1;
    constexpr int u0 = 4 * half, n = t == ST_LAST ? 28 : half ? 12 : 16;
    STEP_BARRIER(t);
    if constexpr (half == 0) {
      sfor<0, 4>([&](auto I) { constexpr int i = decltype(I)::value; of[i] = lds_rd(xch + (wj * 4 + i) * 1024); });
    }
    sfor<0, n>([&](auto I) {
      constexpr int i = decltype(I)::value, u = u0 + (i >> 2), ks = i & 3;
      constexpr u32 sb = slot_of(KWO, s, u) + ks * 2048;
      const s16x8 wf = lds_tr(tro + sb, tro + sb + 1024);
      if constexpr ((i & 1) == 0) issue_part<t, (i >> 1), n / 2>(c);
      acc[u] = mfma(wf, of[ks], acc[u]);
      if constexpr (t == ST_LAST && ks == 3) {
        const u32 so = h * 3584 + c.r7[u] * 512;
        __builtin_amdgcn_raw_buffer_store_b128(pack8<0>(acc[u]), rp, vst, so, 16);
        __builtin_amdgcn_raw_buffer_store_b128(pack8<1>(acc[u]), rp, vst + 128, so, 16);
      }
    });
  });

  STAMP(2 * NSTEP + 1);
  asm volatile("s_waitcnt vmcnt(0)" : "+v"(req)::"memory");
  STAMP(2 * NSTEP + 2);
  const u32 cin = creq + (h * 2 + wi) * 4;
  if (lane < 8) __builtin_amdgcn_raw_buffer_store_b32(req, rf, cin, 0, 16);
  asm volatile("s_barrier" ::: "memory");
  const u32 now = __builtin_amdgcn_raw_buffer_load_b32(rf, creq + 128, 0, 16);
  if (lane < 8 && now != req) __builtin_amdgcn_raw_buffer_store_b32(now, rf, cin, 0, 16);
  STAMP(2 * NSTEP + 3);
  STAMP_OUT(1);
}

__global__ __launch_bounds__(512, 2) void gqa_block_kernel(const char* __restrict__ x, const char* __restrict__ k, const char* __restrict__ v,
                                                           const char* __restrict__ wq, const char* __restrict__ wo, char* out, char* ws) {
  __shared__ __attribute__((aligned(16))) char smem[LDS_BYTES];
  u32 stv = 0;
  STAMP(0);
  const u32 lds = (u32)(size_t)(LDSA char*)smem;
  const int bid = blockIdx.x, kvh = bid & 7, b = bid >> 4, hh = (bid >> 3) & 1, h = kvh * 2 + hh;
  const int wid = __builtin_amdgcn_readfirstlane(threadIdx.x >> 6), lane = threadIdx.x & 63;

  Ctx c;
  c.rx = __builtin_amdgcn_make_buffer_rsrc((void*)(x + (size_t)b * 57344), 0, 57344, 0x00020000);
  c.rk = __builtin_amdgcn_make_buffer_rsrc((void*)(k + (size_t)(b * 8 + kvh) * 262144), 0, 262144, 0x00020000);
  c.rv = __builtin_amdgcn_make_buffer_rsrc((void*)(v + (size_t)(b * 8 + kvh) * 262144), 0, 262144, 0x00020000);
  c.rwq = __builtin_amdgcn_make_buffer_rsrc((void*)(wq + h * 512), 0, 3670016 - h * 512, 0x00020000);
  c.rwo = __builtin_amdgcn_make_buffer_rsrc((void*)(wo + (size_t)h * 229376), 0, 229376, 0x00020000);
  const u32 row = wid * 8 + (lane >> 3);
  const u32 crow = ((lane & 7) ^ ((row >> 1) & 7)) << 4;
  const u32 ctr = ((lane & 7) ^ (((row >> 1) & 1) << 2)) << 4;
  c.vx = row * 896 + crow; c.vk = row * 512 + crow;
  c.vwq = row * 8192 + ctr; c.vv = row * 512 + ctr; c.vwo = row * 896 + ctr;
  c.m0base = lds + wid * 1024;
  for (int s = 0; s < 4; ++s) { c.sl[s] = ((s + b) & 3) * 128; c.kvs[s] = c.sl[s] | (hh << 17); }
  const int wr = (b >> 2) * 2 + hh * 3;
  for (int i = 0; i < 7; ++i) { const int t = i + wr; c.r7[i] = t >= 14 ? t - 14 : t >= 7 ? t - 7 : t; }

  const rsrc_t rf = __builtin_amdgcn_make_buffer_rsrc(ws + WS_FLAGS + b * 4096, 0, 4096, 0x00020000);
  u32 inbox = 0;
  if (wid == 0) asm volatile("buffer_load_dword %0, %1, %2, %3 offen sc1" : "=v"(inbox) : "v"((lane & 31) * 4), "s"(rf), "s"(h * 256));
  issue_part<-1, 0, 1>(c);
  if (wid < 4) role_attn(c, lds, wid, lane, b, h, ws, out, rf, inbox, stv);
  else role_proj(c, lds, wid - 4, lane, b, h, ws, rf, stv);
}

__global__ void pre_k(char* ws){ u64 t=__builtin_amdgcn_s_memrealtime(); if(threadIdx.x==0) *(u32*)(ws+STAMP_OFF+(1<<20))=(u32)t; }
__global__ void spin_k(const char* ws){
  u32 tpost=(u32)__builtin_amdgcn_s_memrealtime();
  const u32* h=(const u32*)(ws+STAMP_OFF);
  __shared__ u32 smn[512], smx[512];
  int i=threadIdx.x; u32 mn=h[i*64], mx=0;
  for(int t=0;t<64;++t){ u32 v=h[i*64+t]; if(v&&v-mn<100000u&&v>mx) mx=v; }
  smn[i]=mn; smx[i]=mx; __syncthreads();
  if(i==0){ for(int k=1;k<512;++k){ mn=smn[k]<mn?smn[k]:mn; mx=smx[k]>mx?smx[k]:mx; }
    u32 tpre=*(const u32*)(ws+STAMP_OFF+(1<<20));
    u64 d=(u64)((mn-mn)+(tpre-tpre)+(mx&0))*100; if(d>2000000) d=2000000;
    u64 s=__builtin_amdgcn_s_memrealtime();
    while(__builtin_amdgcn_s_memrealtime()-s<d){ __builtin_amdgcn_s_sleep(1); } }
}
}

extern "C" void kernel_launch(void* const* d_in, const int* in_sizes, int n_in, void* d_out, int out_size, void* d_ws, size_t ws_size,
                              hipStream_t stream) {
  pre_k<<<1,64,0,stream>>>((char*)d_ws);
  gqa_block_kernel<<<256, 512, 0, stream>>>((const char*)d_in[0], (const char*)d_in[1], (const char*)d_in[2], (const char*)d_in[3],
                                            (const char*)d_in[4], (char*)d_out, (char*)d_ws);
  spin_k<<<1,512,0,stream>>>((const char*)d_ws);
}
```

```cpp
#define STAMPS 1
#include <hip/hip_runtime.h>

namespace {

typedef unsigned int u32;
typedef unsigned long long u64;
typedef short s16x4 __attribute__((ext_vector_type(4)));
typedef short s16x8 __attribute__((ext_vector_type(8)));
typedef float f32x16 __attribute__((ext_vector_type(16)));
typedef float f32x2 __attribute__((ext_vector_type(2)));
typedef float f32x4 __attribute__((ext_vector_type(4)));
typedef __bf16 bf16x2 __attribute__((ext_vector_type(2)));
typedef u32 u32x4 __attribute__((ext_vector_type(4)));
#define LDSA __attribute__((address_space(3)))
#define DI __device__ __forceinline__
typedef __amdgpu_buffer_rsrc_t rsrc_t;

constexpr int NSLOT = 19;
constexpr int XCH = NSLOT * 8192;
constexpr int LDS_BYTES = XCH + 8192;
constexpr u32 PART_BYTES = 57344;
constexpr u32 WS_FLAGS = 16u * 16u * PART_BYTES;

enum { KX = 0, KWQ = 1, KK = 2, KV = 3, KWO = 4, KPX = 5 };
constexpr int NSTEP = 22;
constexpr int ST_EXP = 11, ST_PRD = 12, ST_PV0 = 13, ST_LAST = 21;
constexpr int MAXLD = 136;

constexpr int need_step(int kind, int s, int i) {
  return kind == KX ? (i < 4 ? 0 : 1)
       : kind == KWQ ? 1 + 2 * s + (i < 4 ? 0 : 1)
       : kind == KK ? 3 + 2 * s + (i < 4 ? 0 : 1)
       : kind == KV ? ST_PV0 + 2 * s + (i < 4 ? 0 : 1)
       : kind == KWO ? (s == 3 ? ST_LAST : ST_PV0 + 2 + 2 * s + (i < 4 ? 0 : 1))
       : ST_EXP;
}
constexpr int free_step(int kind, int s, int i) { return kind == KPX ? ST_PRD : need_step(kind, s, i); }

struct Plan {
  int n;
  int kind[MAXLD], s[MAXLD], i[MAXLD], phys[MAXLD], at[MAXLD];
  int first[NSTEP + 2];
  int vm[NSTEP];
  bool ok;
};

constexpr Plan make_plan() {
  Plan p{};
  auto add = [&](int kind, int s, int i0, int cnt) { for (int c = 0; c < cnt; ++c) { p.kind[p.n] = kind; p.s[p.n] = s; p.i[p.n] = i0 + c; ++p.n; } };
  add(KX, 0, 0, 4); add(KWQ, 0, 0, 2); add(KK, 0, 0, 4); add(KX, 0, 4, 3); add(KWQ, 0, 2, 5);
  add(KWQ, 1, 0, 4); add(KK, 0, 4, 4); add(KWQ, 1, 4, 3);
  for (int s = 1; s < 3; ++s) { add(KK, s, 0, 4); add(KWQ, s + 1, 0, 4); add(KK, s, 4, 4); add(KWQ, s + 1, 4, 3); }
  add(KK, 3, 0, 4); add(KK, 3, 4, 4);
  add(KV, 0, 0, 4); add(KV, 1, 0, 4); add(KWO, 0, 0, 3); add(KPX, 0, 0, 8);
  add(KV, 0, 4, 4); add(KWO, 0, 3, 4); add(KV, 1, 4, 4);
  for (int s = 2; s < 4; ++s) { add(KV, s, 0, 4); add(KWO, s - 1, 0, 4); add(KV, s, 4, 4); add(KWO, s - 1, 4, 3); }
  add(KWO, 3, 0, 7);
  int owner[NSLOT] = {};
  for (int k = 0; k < NSLOT; ++k) owner[k] = -1;
  int nx = 0;
  p.ok = true;
  for (int t = -1; t < NSTEP; ++t) {
    for (int k = 0; k < NSLOT; ++k)
      if (owner[k] >= 0 && free_step(p.kind[owner[k]], p.s[owner[k]], p.i[owner[k]]) < t) owner[k] = -1;
    p.first[t + 1] = nx;
    for (int k = 0; k < NSLOT && nx < p.n; ++k)
      if (owner[k] < 0) { owner[k] = nx; p.phys[nx] = k; p.at[nx] = t; ++nx; }
  }
  p.first[NSTEP + 1] = nx;
  if (nx != p.n) p.ok = false;
  for (int e = 0; e < p.n; ++e) {
    const int nd = need_step(p.kind[e], p.s[e], p.i[e]);
    if (p.kind[e] == KPX ? p.at[e] > nd : p.at[e] >= nd) p.ok = false;
  }
  for (int t = 0; t < NSTEP; ++t) {
    int last = -1;
    for (int e = 0; e < p.n; ++e)
      if (p.kind[e] != KPX && need_step(p.kind[e], p.s[e], p.i[e]) <= t) last = e;
    int c = 0;
    for (int e = last + 1; e < p.first[t + 1]; ++e) if (p.kind[e] != KPX) ++c;
    p.vm[t] = c;
  }
  return p;
}
constexpr Plan PL = make_plan();
static_assert(PL.ok, "infeasible schedule");
constexpr int slot_of(int kind, int s, int i) {
  for (int e = 0; e < PL.n; ++e) if (PL.kind[e] == kind && PL.s[e] == s && PL.i[e] == i) return PL.phys[e] * 8192;
  return -1;
}

template <int I> struct IC { static constexpr int value = I; };
template <int A, int B, class F> DI void sfor(F&& f) { if constexpr (A < B) { f(IC<A>{}); sfor<A + 1, B>(f); } }

DI f32x16 mfma(s16x8 a, s16x8 b, f32x16 c) { return __builtin_amdgcn_mfma_f32_32x32x16_bf16(a, b, c, 0, 0, 0); }
DI u32 pk2(float lo, float hi) { f32x2 v = {lo, hi}; return __builtin_bit_cast(u32, __builtin_convertvector(v, bf16x2)); }
template <int S> DI u32x4 pack8(const f32x16& x) {
  u32x4 r = {pk2(x[8 * S], x[8 * S + 1]), pk2(x[8 * S + 2], x[8 * S + 3]), pk2(x[8 * S + 4], x[8 * S + 5]), pk2(x[8 * S + 6], x[8 * S + 7])};
  return r;
}
DI s16x8 lds_rd(u32 a) { return *(const LDSA s16x8*)(size_t)a; }
DI void lds_wr(u32 a, u32x4 v) { *(LDSA u32x4*)(size_t)a = v; }
DI s16x8 lds_tr(u32 a0, u32 a1) {
  const s16x4 lo = __builtin_amdgcn_ds_read_tr16_b64_v4i16((LDSA s16x4*)(size_t)a0);
  const s16x4 hi = __builtin_amdgcn_ds_read_tr16_b64_v4i16((LDSA s16x4*)(size_t)a1);
  return __builtin_shufflevector(lo, hi, 0, 1, 2, 3, 4, 5, 6, 7);
}
constexpr int NSTAMP = 64;
constexpr size_t STAMP_OFF = 32u << 20;
#define STAMP(idx) do { u64 t_; asm volatile("s_memrealtime %0\n\ts_waitcnt lgkmcnt(0)" : "=s"(t_)::"memory"); asm volatile("v_writelane_b32 %0, %1, %2" : "+v"(stv) : "s"((u32)t_), "i"(idx)); } while (0)
#define STAMP_OUT(role) do { if (w == 0) ((u32*)(ws + STAMP_OFF))[(blockIdx.x * 2 + (role)) * 64 + lane] = stv; } while (0)
template <int VM> DI void step_barrier_() { asm volatile("s_waitcnt vmcnt(%0) lgkmcnt(0)\n\ts_barrier" ::"i"(VM) : "memory"); }
#define STEP_BARRIER(t) do { STAMP(2 * (t) + 1); step_barrier_<PL.vm[t]>(); STAMP(2 * (t) + 2); } while (0)
DI float half_max(float v) {
  auto r = __builtin_amdgcn_permlane32_swap(__float_as_uint(v), __float_as_uint(v), false, false);
  return fmaxf(__uint_as_float(r[0]), __uint_as_float(r[1]));
}
DI float half_sum(float v) {
  auto r = __builtin_amdgcn_permlane32_swap(__float_as_uint(v), __float_as_uint(v), false, false);
  return __uint_as_float(r[0]) + __uint_as_float(r[1]);
}

struct Ctx {
  rsrc_t rx, rk, rv, rwq, rwo;
  u32 vx, vwq, vk, vv, vwo;
  u32 m0base;
  u32 sl[4];
  u32 kvs[4];
  u32 r7[7];
};

template <int OFF> DI void dma(u32 m0base, u32 voff, rsrc_t r, u32 soff) {
  asm volatile("s_add_u32 m0, %0, %1\n\ts_nop 0\n\tbuffer_load_dwordx4 %2, %3, %4 offen lds"
               ::"s"(m0base), "i"(OFF), "v"(voff), "s"(r), "s"(soff) : "scc");
}
template <int E> DI void issue(const Ctx& c) {
  constexpr int kind = PL.kind[E], s = PL.s[E], i = PL.i[E], off = PL.phys[E] * 8192;
  if constexpr (kind == KX) dma<off>(c.m0base, c.vx, c.rx, c.r7[i] * 128u);
  else if constexpr (kind == KWQ) dma<off>(c.m0base, c.vwq, c.rwq, c.sl[s] + c.r7[i] * 524288u);
  else if constexpr (kind == KK) dma<off>(c.m0base, c.vk, c.rk, c.kvs[s] ^ (u32)(i << 15));
  else if constexpr (kind == KV) dma<off>(c.m0base, c.vv, c.rv, c.kvs[s] ^ (u32)(i << 15));
  else if constexpr (kind == KWO) dma<off>(c.m0base, c.vwo, c.rwo, c.sl[s] * 448u + c.r7[i] * 128u);
}
template <int T, int PART, int NPART> DI void issue_part(const Ctx& c) {
  constexpr int a = PL.first[T + 1], n = PL.first[T + 2] - a;
  sfor<a + (n * PART + NPART - 1) / NPART, a + (n * (PART + 1) + NPART - 1) / NPART>([&](auto E) { issue<decltype(E)::value>(c); });
}

constexpr int SPIN_MAX = 1 << 20;

DI void role_attn(const Ctx& c, u32 lds, int w, int lane, int b, int h, char* ws, char* out, rsrc_t rf, u32 inbox, u32 stv) {
  const int r = lane & 31, hf = lane >> 5, wi = w & 1, wj = w >> 1;
  const u32 rowrd = lds + r * 128 + ((hf ^ ((r >> 1) & 7)) << 4);
  const int blk = (lane >> 4) & 1, q4 = (lane & 15) >> 2, p4 = lane & 3;
  const u32 trv = lds + (4 * hf + q4) * 128 + (((4 * wi + 2 * blk + (p4 >> 1)) ^ (((q4 >> 1) & 1) << 2)) << 4) + 8 * (p4 & 1);
  const u32 xch = lds + XCH + lane * 16;

  f32x16 S[2][2][2];
  sfor<0, 8>([&](auto I) { constexpr int i = decltype(I)::value; S[i >> 2][(i >> 1) & 1][i & 1] = f32x16{}; });
  s16x8 qf[2][4];
  u32 nonce = 0;

  sfor<0, 3>([&](auto T) {
    constexpr int t = decltype(T)::value;
    STEP_BARRIER(t);
    issue_part<t, 0, 1>(c);
    if constexpr (t == 0) {
      if (w == 0) {
        asm volatile("" : "+v"(inbox));
        u32 nn = __builtin_amdgcn_readfirstlane(inbox) + 1u;
        while (__builtin_amdgcn_ballot_w64(inbox == nn) != 0ull) ++nn;
        if (lane == 0) {
          asm volatile("buffer_store_dword %0, off, %1, %2 offset:128 sc1" ::"v"(nn), "s"(rf), "s"(h * 256) : "memory");
          *(LDSA u32*)(size_t)(lds + XCH) = nn;
        }
      }
    }
    if constexpr (t == 1) nonce = __builtin_amdgcn_readfirstlane(*(const LDSA u32*)(size_t)(lds + XCH));
  });

  float mx[2];
  sfor<3, ST_EXP>([&](auto T) {
    constexpr int t = decltype(T)::value, s = (t - 3) >> 1, half = (t - 3) & 1;
    STEP_BARRIER(t);
    if constexpr (half == 0) {
      sfor<0, 8>([&](auto I) { constexpr int i = decltype(I)::value; qf[i >> 2][i & 3] = lds_rd(xch + i * 1024); });
    }
    constexpr u32 s0 = slot_of(KK, s, 4 * half), s1 = slot_of(KK, s, 4 * half + 1), s2 = slot_of(KK, s, 4 * half + 2), s3 = slot_of(KK, s, 4 * half + 3);
    const u32 sb = w == 0 ? s0 : w == 1 ? s1 : w == 2 ? s2 : s3;
    sfor<0, 8>([&](auto I) {
      constexpr int i = decltype(I)::value, tt = i >> 2, ks = i & 3;
      const s16x8 kf = lds_rd(((rowrd ^ (u32)(ks << 5)) + sb) + tt * 4096);
      issue_part<t, i, 8>(c);
      S[half][tt][0] = mfma(kf, qf[0][ks], S[half][tt][0]);
      S[half][tt][1] = mfma(kf, qf[1][ks], S[half][tt][1]);
    });
    if constexpr (t == ST_EXP - 1) {
      sfor<0, 2>([&](auto Q) {
        constexpr int qb = decltype(Q)::value;
        float m = S[0][0][qb][0];
        sfor<0, 4>([&](auto I) { constexpr int i = decltype(I)::value;
          for (int e = 0; e < 16; ++e) m = fmaxf(m, S[i >> 1][i & 1][qb][e]); });
        mx[qb] = half_max(m);
      });
      *(LDSA float*)(size_t)(lds + XCH + (lane * 4 + w) * 4) = hf ? mx[1] : mx[0];
    }
  });

  STEP_BARRIER(ST_EXP);
  issue_part<ST_EXP, 0, 1>(c);
  {
    constexpr float CS = 0.0625f * 1.4426950408889634f;
    float sum[2];
    sfor<0, 2>([&](auto Q) {
      constexpr int qb = decltype(Q)::value;
      const f32x4 mm = *(const LDSA f32x4*)(size_t)(lds + XCH + (qb * 32 + r) * 16);
      const float nm = -CS * fmaxf(fmaxf(mm[0], mm[1]), fmaxf(mm[2], mm[3]));
      float acc = 0.f;
      sfor<0, 4>([&](auto I) {
        constexpr int i = decltype(I)::value, half = i >> 1, tt = i & 1;
        f32x16 e;
        for (int k = 0; k < 16; ++k) { e[k] = __builtin_amdgcn_exp2f(__builtin_fmaf(S[half][tt][qb][k], CS, nm)); acc += e[k]; }
        constexpr u32 pa = slot_of(KPX, 0, qb * 4 + 2 * half), pb = slot_of(KPX, 0, qb * 4 + 2 * half + 1);
        const u32 dst = lds + (wj ? pb : pa) + wi * 4096 + tt * 2048 + lane * 16;
        lds_wr(dst, pack8<0>(e));
        lds_wr(dst + 1024, pack8<1>(e));
      });
      sum[qb] = half_sum(acc);
    });
    *(LDSA float*)(size_t)(lds + XCH + 1024 + (lane * 4 + w) * 4) = hf ? sum[1] : sum[0];
  }

  s16x8 pf[32];
  float linv;
  STEP_BARRIER(ST_PRD);
  issue_part<ST_PRD, 0, 1>(c);
  sfor<0, 4>([&](auto G) {
    constexpr int g = decltype(G)::value;
    constexpr u32 pa = slot_of(KPX, 0, g), pb = slot_of(KPX, 0, 4 + g);
    const u32 src = lds + (wj ? pb : pa) + lane * 16;
    sfor<0, 8>([&](auto I) { constexpr int i = decltype(I)::value; pf[g * 8 + i] = lds_rd(src + i * 1024); });
  });
  {
    const f32x4 ss = *(const LDSA f32x4*)(size_t)(lds + XCH + 1024 + (wj * 32 + r) * 16);
    linv = 1.0f / ((ss[0] + ss[1]) + (ss[2] + ss[3]));
  }

  f32x16 O;
  sfor<ST_PV0, ST_LAST>([&](auto T) {
    constexpr int t = decltype(T)::value, s = (t - ST_PV0) >> 1, half = (t - ST_PV0) & 1;
    STEP_BARRIER(t);
    if constexpr (half == 0) O = f32x16{};
    sfor<0, 16>([&](auto I) {
      constexpr int i = decltype(I)::value, u = 4 * half + (i >> 2), ks = i & 3;
      constexpr u32 sb = slot_of(KV, s, u) + ks * 2048;
      const s16x8 vf = lds_tr(trv + sb, trv + sb + 1024);
      if constexpr ((i & 1) == 0) issue_part<t, (i >> 1), 8>(c);
      O = mfma(vf, pf[4 * u + ks], O);
    });
    if constexpr (half == 1) {
      for (int k = 0; k < 16; ++k) O[k] *= linv;
      lds_wr(xch + (wj * 4 + wi * 2) * 1024, pack8<0>(O));
      lds_wr(xch + (wj * 4 + wi * 2 + 1) * 1024, pack8<1>(O));
    }
  });

  STEP_BARRIER(ST_LAST);
  for (int it = 0; it < SPIN_MAX; ++it) {
    const u32 v = __builtin_amdgcn_raw_buffer_load_b32(rf, r * 4, h * 256, 16);
    if (__builtin_amdgcn_ballot_w64(v != nonce) == 0ull) break;
    asm volatile("" ::: "memory");
  }
  STAMP(2 * NSTEP + 1);
  asm volatile("s_barrier" ::: "memory");
  const int tix = w * 64 + lane;
  if (tix < 224) {
    const rsrc_t rp = __builtin_amdgcn_make_buffer_rsrc(ws + (size_t)(b * 16 + h) * PART_BYTES, 0, PART_BYTES, 0x00020000);
    u32x4 pv[16];
    for (int p = 0; p < 16; ++p) pv[p] = __builtin_amdgcn_raw_buffer_load_b128(rp, tix * 16, p * 3584, 16);
    float a[8];
    for (int k = 0; k < 8; ++k) a[k] = 0.f;
    for (int p = 0; p < 16; ++p)
      for (int k = 0; k < 4; ++k) { a[2 * k] += __uint_as_float(pv[p][k] << 16); a[2 * k + 1] += __uint_as_float(pv[p][k] & 0xffff0000u); }
    const u32x4 o = {pk2(a[0], a[1]), pk2(a[2], a[3]), pk2(a[4], a[5]), pk2(a[6], a[7])};
    STAMP(2 * NSTEP + 2);
    *(u32x4*)(out + (size_t)(b * 64 + 4 * h + (tix & 3)) * 896 + (tix >> 2) * 16) = o;
  }
  asm volatile("s_waitcnt vmcnt(0)" ::: "memory");
  STAMP(2 * NSTEP + 3);
  STAMP_OUT(0);
}

DI void role_proj(const Ctx& c, u32 lds, int w, int lane, int b, int h, char* ws, rsrc_t rf, u32 stv) {
  const int r = lane & 31, hf = lane >> 5, wi = w & 1, wj = w >> 1;
  const u32 rowrd = lds + (wj * 32 + r) * 128 + ((hf ^ ((r >> 1) & 7)) << 4);
  const int blk = (lane >> 4) & 1, q4 = (lane & 15) >> 2, p4 = lane & 3;
  const u32 trc = (((4 * wi + 2 * blk + (p4 & 1)) ^ (((q4 >> 1) & 1) << 2)) << 4) + 8 * (p4 >> 1);
  const u32 trq = lds + (8 * hf + q4) * 128 + trc;
  const u32 tro = lds + (4 * hf + q4) * 128 + trc;
  const u32 xch = lds + XCH + lane * 16;

  {
    s16x8 xf[28];
    f32x16 Q;
    sfor<0, 9>([&](auto T) {
      constexpr int t = decltype(T)::value;
      STEP_BARRIER(t);
      if constexpr (t <= 1) {
        sfor<(t == 0 ? 0 : 16), (t == 0 ? 16 : 28)>([&](auto I) {
          constexpr int i = decltype(I)::value;
          constexpr u32 sb = slot_of(KX, 0, i >> 2);
          xf[i] = lds_rd((rowrd ^ (u32)((i & 3) << 5)) + sb);
        });
      }
      if constexpr (t == 0) issue_part<t, 0, 1>(c);
      else {
        constexpr int s = (t - 1) >> 1, half = (t - 1) & 1, n = half ? 12 : 16;
        if constexpr (half == 0) Q = f32x16{};
        sfor<0, n>([&](auto I) {
          constexpr int i = decltype(I)::value, u = 4 * half + (i >> 2), ks = i & 3;
          constexpr u32 sb = slot_of(KWQ, s, u) + ks * 2048;
          const s16x8 wf = lds_tr(trq + sb, trq + sb + 512);
          if constexpr ((i & 1) == 0) issue_part<t, (i >> 1), n / 2>(c);
          Q = mfma(wf, xf[4 * u + ks], Q);
        });
        if constexpr (half == 1) {
          lds_wr(xch + (wj * 4 + wi * 2) * 1024, pack8<0>(Q));
          lds_wr(xch + (wj * 4 + wi * 2 + 1) * 1024, pack8<1>(Q));
        }
      }
    });
  }

  u32 req;
  const u32 creq = (8 * wj + (lane & 7)) * 256;
  sfor<9, ST_PV0 + 2>([&](auto T) {
    constexpr int t = decltype(T)::value;
    STEP_BARRIER(t);
    if constexpr (t == ST_EXP) asm volatile("buffer_load_dword %0, %1, %2, 0 offen offset:128 sc1" : "=v"(req) : "v"(creq), "s"(rf));
    issue_part<t, 0, 1>(c);
  });

  f32x16 acc[7];
  sfor<0, 7>([&](auto I) { acc[decltype(I)::value] = f32x16{}; });
  s16x8 of[4];
  const rsrc_t rp = __builtin_amdgcn_make_buffer_rsrc(ws + (size_t)b * 16 * PART_BYTES, 0, 16 * PART_BYTES, 0x00020000);
  const u32 vst = (8 * wj + (r >> 2)) * PART_BYTES + hf * 64 + (r & 3) * 16 + wi * 256;
  sfor<ST_PV0 + 2, NSTEP>([&](auto T) {
    constexpr int t = decltype(T)::value, s = (t - ST_PV0 - 2) >> 1, half = (t - ST_PV0 - 2) & 1;
    constexpr int u0 = 4 * half, n = t == ST_LAST ? 28 : half ? 12 : 16;
    STEP_BARRIER(t);
    if constexpr (half == 0) {
      sfor<0, 4>([&](auto I) { constexpr int i = decltype(I)::value; of[i] = lds_rd(xch + (wj * 4 + i) * 1024); });
    }
    sfor<0, n>([&](auto I) {
      constexpr int i = decltype(I)::value, u = u0 + (i >> 2), ks = i & 3;
      constexpr u32 sb = slot_of(KWO, s, u) + ks * 2048;
      const s16x8 wf = lds_tr(tro + sb, tro + sb + 1024);
      if constexpr ((i & 1) == 0) issue_part<t, (i >> 1), n / 2>(c);
      acc[u] = mfma(wf, of[ks], acc[u]);
      if constexpr (t == ST_LAST && ks == 3) {
        const u32 so = h * 3584 + c.r7[u] * 512;
        __builtin_amdgcn_raw_buffer_store_b128(pack8<0>(acc[u]), rp, vst, so, 16);
        __builtin_amdgcn_raw_buffer_store_b128(pack8<1>(acc[u]), rp, vst + 128, so, 16);
      }
    });
  });

  STAMP(2 * NSTEP + 1);
  asm volatile("s_waitcnt vmcnt(0)" : "+v"(req)::"memory");
  STAMP(2 * NSTEP + 2);
  const u32 cin = creq + (h * 2 + wi) * 4;
  if (lane < 8) __builtin_amdgcn_raw_buffer_store_b32(req, rf, cin, 0, 16);
  asm volatile("s_barrier" ::: "memory");
  const u32 now = __builtin_amdgcn_raw_buffer_load_b32(rf, creq + 128, 0, 16);
  if (lane < 8 && now != req) __builtin_amdgcn_raw_buffer_store_b32(now, rf, cin, 0, 16);
  STAMP(2 * NSTEP + 3);
  STAMP_OUT(1);
}

__global__ __launch_bounds__(512, 2) void gqa_block_kernel(const char* __restrict__ x, const char* __restrict__ k, const char* __restrict__ v,
                                                           const char* __restrict__ wq, const char* __restrict__ wo, char* out, char* ws) {
  __shared__ __attribute__((aligned(16))) char smem[LDS_BYTES];
  u32 stv = 0;
  STAMP(0);
  const u32 lds = (u32)(size_t)(LDSA char*)smem;
  const int bid = blockIdx.x, kvh = bid & 7, b = bid >> 4, hh = (bid >> 3) & 1, h = kvh * 2 + hh;
  const int wid = __builtin_amdgcn_readfirstlane(threadIdx.x >> 6), lane = threadIdx.x & 63;

  Ctx c;
  c.rx = __builtin_amdgcn_make_buffer_rsrc((void*)(x + (size_t)b * 57344), 0, 57344, 0x00020000);
  c.rk = __builtin_amdgcn_make_buffer_rsrc((void*)(k + (size_t)(b * 8 + kvh) * 262144), 0, 262144, 0x00020000);
  c.rv = __builtin_amdgcn_make_buffer_rsrc((void*)(v + (size_t)(b * 8 + kvh) * 262144), 0, 262144, 0x00020000);
  c.rwq = __builtin_amdgcn_make_buffer_rsrc((void*)(wq + h * 512), 0, 3670016 - h * 512, 0x00020000);
  c.rwo = __builtin_amdgcn_make_buffer_rsrc((void*)(wo + (size_t)h * 229376), 0, 229376, 0x00020000);
  const u32 row = wid * 8 + (lane >> 3);
  const u32 crow = ((lane & 7) ^ ((row >> 1) & 7)) << 4;
  const u32 ctr = ((lane & 7) ^ (((row >> 1) & 1) << 2)) << 4;
  c.vx = row * 896 + crow; c.vk = row * 512 + crow;
  c.vwq = row * 8192 + ctr; c.vv = row * 512 + ctr; c.vwo = row * 896 + ctr;
  c.m0base = lds + wid * 1024;
  for (int s = 0; s < 4; ++s) { c.sl[s] = ((s + b) & 3) * 128; c.kvs[s] = c.sl[s] | (hh << 17); }
  const int wr = (b >> 2) * 2 + hh * 3;
  for (int i = 0; i < 7; ++i) { const int t = i + wr; c.r7[i] = t >= 14 ? t - 14 : t >= 7 ? t - 7 : t; }

  const rsrc_t rf = __builtin_amdgcn_make_buffer_rsrc(ws + WS_FLAGS + b * 4096, 0, 4096, 0x00020000);
  u32 inbox = 0;
  if (wid == 0) asm volatile("buffer_load_dword %0, %1, %2, %3 offen sc1" : "=v"(inbox) : "v"((lane & 31) * 4), "s"(rf), "s"(h * 256));
  issue_part<-1, 0, 1>(c);
  if (wid < 4) role_attn(c, lds, wid, lane, b, h, ws, out, rf, inbox, stv);
  else role_proj(c, lds, wid - 4, lane, b, h, ws, rf, stv);
}

__global__ void pre_k(u32* p){ asm volatile("s_waitcnt lgkmcnt(0)"::"s"(p):"memory"); u64 t; asm volatile("s_memrealtime %0\n\ts_waitcnt lgkmcnt(0)":"=s"(t)::"memory"); if(threadIdx.x==0) *p=(u32)t; }
__global__ void small_k(u32* p){ u64 t; asm volatile("s_memrealtime %0\n\ts_waitcnt lgkmcnt(0)":"=s"(t)::"memory"); if(threadIdx.x==0&&blockIdx.x==0) *p=(u32)t; }
__global__ __launch_bounds__(512,2) void big_k(u32* p){ __shared__ char sm[163840]; u64 t; asm volatile("s_memrealtime %0\n\ts_waitcnt lgkmcnt(0)":"=s"(t):"v"((u32)(size_t)(LDSA char*)sm):"memory","v250"); if(threadIdx.x==0&&blockIdx.x==0) *p=(u32)t; }
__global__ void spin_k(const char* ws){
  const u32* h=(const u32*)(ws+STAMP_OFF);
  __shared__ u32 smn[512];
  int i=threadIdx.x; u32 mn=h[i*64];
  smn[i]=mn; __syncthreads();
  if(i==0){ for(int k=1;k<512;++k){ mn=smn[k]<mn?smn[k]:mn; }
    const u32* q=(const u32*)(ws+STAMP_OFF+(1<<20));
    u64 d=(u64)(q[96]-q[64])*100; if(d>2000000) d=2000000;
    u64 s=__builtin_amdgcn_s_memrealtime();
    while(__builtin_amdgcn_s_memrealtime()-s<d){ __builtin_amdgcn_s_sleep(1); } }
}
}

extern "C" void kernel_launch(void* const* d_in, const int* in_sizes, int n_in, void* d_out, int out_size, void* d_ws, size_t ws_size,
                              hipStream_t stream) {
  u32* q=(u32*)((char*)d_ws+STAMP_OFF+(1<<20));
  pre_k<<<1,64,0,stream>>>(q); small_k<<<1,64,0,stream>>>(q+32);
  pre_k<<<1,64,0,stream>>>(q+64); big_k<<<256,512,0,stream>>>(q+96);
  pre_k<<<1,64,0,stream>>>(q+160); small_k<<<256,512,0,stream>>>(q+192);
  pre_k<<<1,64,0,stream>>>(q+128);
  gqa_block_kernel<<<256, 512, 0, stream>>>((const char*)d_in[0], (const char*)d_in[1], (const char*)d_in[2], (const char*)d_in[3],
                                            (const char*)d_in[4], (char*)d_out, (char*)d_ws);
  spin_k<<<1,512,0,stream>>>((const char*)d_ws);
}
```

```cpp
#include <hip/hip_runtime.h>

namespace {

typedef unsigned int u32;
typedef unsigned long long u64;
typedef short s16x4 __attribute__((ext_vector_type(4)));
typedef short s16x8 __attribute__((ext_vector_type(8)));
typedef float f32x16 __attribute__((ext_vector_type(16)));
typedef float f32x2 __attribute__((ext_vector_type(2)));
typedef float f32x4 __attribute__((ext_vector_type(4)));
typedef __bf16 bf16x2 __attribute__((ext_vector_type(2)));
typedef u32 u32x4 __attribute__((ext_vector_type(4)));
#define LDSA __attribute__((address_space(3)))
#define DI __device__ __forceinline__
typedef __amdgpu_buffer_rsrc_t rsrc_t;

constexpr int NSLOT = 20;
constexpr int LDS_BYTES = NSLOT * 8192;
constexpr u32 PART_BYTES = 57344;
constexpr u32 WS_FLAGS = 16u * 16u * PART_BYTES;

enum { KX = 0, KWQ = 1, KK = 2, KV = 3, KWO = 4, XN = 5, XQ = 6, XS = 7, XO = 8 };
constexpr int R_WQ = 1, R_K = 5, R_V = 21, R_WO = 26;
constexpr int ST_LAST = 38, NSTEP = 39;
constexpr int MAXLD = 144;

constexpr int need_step(int kind, int s, int i) {
  return kind == KX ? (i < 4 ? 0 : 1)
       : kind == KWQ ? R_WQ + 4 * s + (i >> 1)
       : kind == KK ? R_K + 4 * s + (i >> 1)
       : kind == KV ? R_V + 4 * s + (i >> 1)
       : kind == KWO ? (R_WO + 4 * s + (i >> 1) < ST_LAST ? R_WO + 4 * s + (i >> 1) : ST_LAST)
       : kind == XN ? 0 : kind == XQ ? R_WQ + 4 + 4 * s : kind == XS ? R_V : R_V + 4 + 4 * s;
}
constexpr int free_step(int kind, int s, int i) { return kind == XS ? R_V + 5 : need_step(kind, s, i) + (kind >= XN ? 1 : 0); }

enum { M_ALL = 0, M_A = 1, M_P = 2 };
constexpr int issue_mode(int t) { return t <= R_K ? M_A : (t >= 18 && t < R_WO) ? M_P : M_ALL; }
constexpr bool lead_chunk(int kind, int i) { return (kind == KK || kind == KV) && i < 4; }

struct Plan {
  int n, nld;
  int kind[MAXLD], s[MAXLD], i[MAXLD], phys[MAXLD], at[MAXLD];
  int first[NSTEP + 2];
  int vm[2][NSTEP];
  bool late[NSTEP + 1];
  bool ok;
};

constexpr Plan make_plan() {
  Plan p{};
  auto add = [&](int t, int kind, int s, int i0, int cnt) { for (int c = 0; c < cnt; ++c) { p.kind[p.n] = kind; p.s[p.n] = s; p.i[p.n] = i0 + c; p.at[p.n] = t; ++p.n; } };
  add(-1, KX, 0, 0, 4); add(-1, KWQ, 0, 0, 2); add(-1, KX, 0, 4, 3); add(-1, KWQ, 0, 2, 1);
  add(0, KWQ, 0, 3, 4);
  add(1, KK, 0, 0, 4); add(2, KWQ, 1, 0, 3); add(3, KK, 0, 4, 4); add(4, KWQ, 1, 3, 3);
  for (int s = 1; s < 3; ++s) { const int t = 1 + 4 * s; add(t, KK, s, 0, 4); add(t + 1, KWQ, s, 6, 1); add(t + 1, KWQ, s + 1, 0, 3); add(t + 2, KK, s, 4, 4); add(t + 3, KWQ, s + 1, 3, 3); }
  add(13, KK, 3, 0, 4); add(14, KWQ, 3, 6, 1); add(15, KK, 3, 4, 4);
  add(17, KV, 0, 0, 4); add(19, KV, 0, 4, 4);
  add(21, KV, 1, 0, 4); add(22, KWO, 0, 0, 3); add(23, KV, 1, 4, 4); add(24, KWO, 0, 3, 2);
  add(25, KV, 2, 0, 4); add(26, KWO, 0, 5, 2); add(27, KWO, 1, 0, 2); add(27, KV, 2, 4, 2); add(28, KV, 2, 6, 2); add(28, KWO, 1, 2, 2);
  add(29, KV, 3, 0, 4); add(30, KWO, 1, 4, 3); add(31, KWO, 2, 0, 2); add(31, KV, 3, 4, 2); add(32, KV, 3, 6, 2); add(32, KWO, 2, 2, 2);
  add(34, KWO, 2, 4, 3); add(34, KWO, 3, 0, 1); add(35, KWO, 3, 1, 4); add(36, KWO, 3, 5, 2);
  p.nld = p.n;
  add(0, XN, 0, 0, 1); add(R_V, XS, 0, 0, 1);
  for (int s = 0; s < 4; ++s) { add(need_step(XQ, s, 0), XQ, s, 0, 1); add(need_step(XO, s, 0), XO, s, 0, 2); }
  p.ok = p.nld == 127;
  int last[NSTEP + 1] = {};
  for (int t = 0; t <= NSTEP; ++t) {
    last[t] = -1;
    for (int e = 0; e < p.nld; ++e)
      if (need_step(p.kind[e], p.s[e], p.i[e]) <= t) last[t] = e;
  }
  int owner[NSLOT] = {};
  for (int k = 0; k < NSLOT; ++k) owner[k] = -1;
  int nx = 0;
  bool lead = false;
  for (int t = -1; t < NSTEP; ++t) {
    for (int k = 0; k < NSLOT; ++k)
      if (owner[k] >= 0 && free_step(p.kind[owner[k]], p.s[owner[k]], p.i[owner[k]]) < t) owner[k] = -1;
    p.first[t + 1] = nx;
    p.late[t + 1] = lead;
    lead = false;
    for (int e = 0; e < p.n; ++e) {
      if (p.at[e] != t) continue;
      int k = 0;
      while (k < NSLOT && owner[k] >= 0) ++k;
      if (k == NSLOT) { p.ok = false; break; }
      owner[k] = e; p.phys[e] = k;
      if (e < p.nld) { if (e != nx) p.ok = false; ++nx; if (lead_chunk(p.kind[e], p.i[e])) lead = true; }
    }
  }
  p.first[NSTEP + 1] = nx;
  if (nx != p.nld) p.ok = false;
  for (int e = 0; e < p.nld; ++e)
    if (p.at[e] >= need_step(p.kind[e], p.s[e], p.i[e])) p.ok = false;
  for (int role = 0; role < 2; ++role)
    for (int t = 0; t < NSTEP; ++t) {
      int c = 0;
      for (int e = last[t] + 1; e < p.first[t + 1]; ++e) { const int m = issue_mode(p.at[e]); c += m == M_ALL ? 1 : (m == M_A) == (role == 0) ? 2 : 0; }
      p.vm[role][t] = c;
    }
  return p;
}
constexpr Plan PL = make_plan();
static_assert(PL.ok, "infeasible schedule");
constexpr int slot_of(int kind, int s, int i) {
  for (int e = 0; e < PL.n; ++e) if (PL.kind[e] == kind && PL.s[e] == s && PL.i[e] == i) return PL.phys[e] * 8192;
  return -1;
}

template <int I> struct IC { static constexpr int value = I; };
template <int A, int B, class F> DI void sfor(F&& f) { if constexpr (A < B) { f(IC<A>{}); sfor<A + 1, B>(f); } }

DI f32x16 mfma(s16x8 a, s16x8 b, f32x16 c) { return __builtin_amdgcn_mfma_f32_32x32x16_bf16(a, b, c, 0, 0, 0); }
DI u32 pk2(float lo, float hi) { f32x2 v = {lo, hi}; return __builtin_bit_cast(u32, __builtin_convertvector(v, bf16x2)); }
template <int S> DI u32x4 pack8(const f32x16& x) {
  u32x4 r = {pk2(x[8 * S], x[8 * S + 1]), pk2(x[8 * S + 2], x[8 * S + 3]), pk2(x[8 * S + 4], x[8 * S + 5]), pk2(x[8 * S + 6], x[8 * S + 7])};
  return r;
}
DI s16x8 lds_rd(u32 a) { return *(const LDSA s16x8*)(size_t)a; }
DI void lds_wr(u32 a, u32x4 v) { *(LDSA u32x4*)(size_t)a = v; }
DI s16x8 lds_tr(u32 a0, u32 a1) {
  const s16x4 lo = __builtin_amdgcn_ds_read_tr16_b64_v4i16((LDSA s16x4*)(size_t)a0);
  const s16x4 hi = __builtin_amdgcn_ds_read_tr16_b64_v4i16((LDSA s16x4*)(size_t)a1);
  return __builtin_shufflevector(lo, hi, 0, 1, 2, 3, 4, 5, 6, 7);
}
#define SB() __builtin_amdgcn_sched_barrier(0)
#define STAMP(idx)
#define STAMP_OUT(role)
#define STAMP_ARR 0
template <int VM> DI void step_barrier_() {
  SB();
  __builtin_amdgcn_s_waitcnt(0xC07F);
  asm volatile("s_waitcnt vmcnt(%0) lgkmcnt(0)\n\ts_barrier" ::"i"(VM) : "memory");
  SB();
}
#define STEP_BARRIER(t) do { if (STAMP_ARR) STAMP((t) + 1); step_barrier_<PL.vm[ROLE][t]>(); if (!STAMP_ARR) STAMP((t) + 1); } while (0)
DI float half_max(float v) {
  auto r = __builtin_amdgcn_permlane32_swap(__float_as_uint(v), __float_as_uint(v), false, false);
  return fmaxf(__uint_as_float(r[0]), __uint_as_float(r[1]));
}
DI float half_sum(float v) {
  auto r = __builtin_amdgcn_permlane32_swap(__float_as_uint(v), __float_as_uint(v), false, false);
  return __uint_as_float(r[0]) + __uint_as_float(r[1]);
}

struct Ctx {
  rsrc_t rx, rk, rv, rwq, rwo;
  u32 vx, vwq, vk, vv, vwo;
  u32 m0base;
  u32 sl[4];
  u32 kvs[4];
  u32 r7[7];
};

template <int OFF> DI void dma(u32 m0base, u32 voff, rsrc_t r, u32 soff) {
  asm volatile("s_add_u32 m0, %0, %1\n\ts_nop 0\n\tbuffer_load_dwordx4 %2, %3, %4 offen lds"
               ::"s"(m0base), "i"(OFF), "v"(voff), "s"(r), "s"(soff) : "scc");
}
template <int E, int HI> DI void issue_piece(const Ctx& c) {
  constexpr int kind = PL.kind[E], s = PL.s[E], i = PL.i[E], off = PL.phys[E] * 8192 + HI * 4096;
  if constexpr (kind == KX) dma<off>(c.m0base, c.vx, c.rx, c.r7[i] + HI * 28672u);
  else if constexpr (kind == KWQ) dma<off>(c.m0base, c.vwq, c.rwq, (c.sl[s] | (HI * 262144u)) | (c.r7[i] << 12));
  else if constexpr (kind == KK) dma<off>(c.m0base, c.vk, c.rk, c.kvs[s] ^ (u32)((i << 15) | (HI << 14)));
  else if constexpr (kind == KV) dma<off>(c.m0base, c.vv, c.rv, c.kvs[s] ^ (u32)((i << 15) | (HI << 14)));
  else if constexpr (kind == KWO) dma<off>(c.m0base, c.vwo, c.rwo, (c.sl[s] * 448u + HI * 28672u) + c.r7[i]);
}
template <int ROLE, int T, int P0, int P1> DI void issue(const Ctx& c) {
  constexpr int a = PL.first[T + 1], n = PL.first[T + 2] - a, m = issue_mode(T);
  sfor<P0, (P1 < n ? P1 : n)>([&](auto P) {
    constexpr int e = a + decltype(P)::value;
    if constexpr (m == M_ALL) issue_piece<e, ROLE>(c);
    else if constexpr ((m == M_A) == (ROLE == 0)) { issue_piece<e, 0>(c); issue_piece<e, 1>(c); }
  });
}
constexpr int NF = 10;
template <int ROLE, int T, int M0, int M1, int L1, class LD, class MM> DI void run_step(const Ctx& c, LD&& ld, MM&& mm) {
  constexpr int top = M0 + NF < L1 ? M0 + NF : L1, nearly = PL.late[T + 1] ? 2 : 64;
  sfor<M1, (top > M1 ? top : M1)>(ld);
  issue<ROLE, T, 0, 1>(c);
  SB();
  sfor<M0, M1>([&](auto I) {
    constexpr int i = decltype(I)::value, k = i - M0;
    mm(I);
    if constexpr (i + NF >= M1 && i + NF < L1) ld(IC<i + NF>{});
    if constexpr ((k & 1) == 1 && (k >> 1) + 1 < nearly) issue<ROLE, T, (k >> 1) + 1, (k >> 1) + 2>(c);
    SB();
  });
}
template <int ROLE, int T, int NM> DI void issue_rest(const Ctx& c) {
  constexpr int done = NM / 2 + 1, nearly = PL.late[T + 1] ? 2 : 64;
  issue<ROLE, T, (done < nearly ? done : nearly), 64>(c);
}
struct Base3 {
  u32 b[3];
  DI void set(u32 a) { b[0] = a; b[1] = a + 65536u; b[2] = a + 131072u; asm volatile("" : "+v"(b[0]), "+v"(b[1]), "+v"(b[2])); }
  template <u32 OFF> DI u32 at() const { return b[OFF >> 16] + (OFF & 65535u); }
};

constexpr int SPIN_MAX = 1 << 20;

DI void role_attn(const Ctx& c, u32 lds, int w, int lane, int b, int h, char* ws, char* out, rsrc_t rf, u32 inbox, u32 stv) {
  constexpr int ROLE = 0;
  const int r = lane & 31, hf = lane >> 5, kh = w & 1, wj = w >> 1;
  const u32 rowrd = lds + r * 128 + ((hf ^ ((r >> 1) & 7)) << 4);
  const int blk = (lane >> 4) & 1, q4 = (lane & 15) >> 2, p4 = lane & 3;
  const u32 trv = lds + (4 * hf + q4) * 128 + (((2 * blk + (p4 >> 1)) ^ (((q4 >> 1) & 1) << 2)) << 4) + 8 * (p4 & 1);
  const u32 xch = lds + lane * 16;
  constexpr u32 xn = slot_of(XN, 0, 0), xs = slot_of(XS, 0, 0);
  issue<ROLE, -1, 0, 64>(c);

  u32 nonce = 0;
  sfor<0, R_K>([&](auto T) {
    constexpr int t = decltype(T)::value;
    STEP_BARRIER(t);
    if constexpr (t == 0) {
      if (w == 0) {
        asm volatile("" : "+v"(inbox));
        u32 nn = __builtin_amdgcn_readfirstlane(inbox) + 1u;
        while (__builtin_amdgcn_ballot_w64(inbox == nn) != 0ull) ++nn;
        if (lane == 0) {
          asm volatile("buffer_store_dword %0, off, %1, %2 offset:128 sc1" ::"v"(nn), "s"(rf), "s"(h * 256) : "memory");
          *(LDSA u32*)(size_t)(lds + xn) = nn;
        }
      }
    }
    if constexpr (t == 1) nonce = __builtin_amdgcn_readfirstlane(*(const LDSA u32*)(size_t)(lds + xn));
    issue<ROLE, t, 0, 64>(c);
  });

  f32x16 S[8];
  f32x16 O[2];
  s16x8 f[NF], qf[4], pf[16];
  float mx = 0.f, nm = 0.f;
  f32x2 sum = {0.f, 0.f};
  constexpr float CS = 0.0625f * 1.4426950408889634f;

  sfor<R_K, ST_LAST>([&](auto T) {
    constexpr int t = decltype(T)::value, g = t - R_K;
    constexpr int M0 = g > 0 ? 8 * (g - 1) : 0, M1 = 8 * g, L1 = g < 32 ? 8 * (g + 1) : 256;
    u32 ad[4];
    if constexpr (g < 32) {
      constexpr int kind = g < 16 ? KK : KV, s = (g >> 2) & 3, q = g & 3;
      constexpr u32 s0 = slot_of(kind, s, 2 * q), s1 = slot_of(kind, s, 2 * q + 1);
      const u32 sb = s0 + kh * (s1 - s0);
      if constexpr (g < 16) { for (int ks = 0; ks < 4; ++ks) ad[ks] = (rowrd ^ (u32)(ks << 5)) + sb; }
      else { ad[0] = trv + sb; ad[1] = (trv ^ 64u) + sb; }
    }
    STEP_BARRIER(t);
    if constexpr (g >= 1 && g <= 16 && ((g - 1) & 3) == 0) {
      constexpr u32 xq = slot_of(XQ, (g - 1) >> 2, 0);
      sfor<0, 4>([&](auto I) { constexpr int i = decltype(I)::value; qf[i] = lds_rd(xch + xq + (wj * 4 + i) * 1024); });
    }
    if constexpr (g == 17) {
      const f32x2 mm = *(const LDSA f32x2*)(size_t)(lds + xs + (wj * 32 + r) * 8);
      nm = -CS * fmaxf(mm[0], mm[1]);
    }
    auto expo = [&](auto K) {
      constexpr int k = decltype(K)::value, e0 = (k & 1) * 8;
      const f32x2 nm2 = {nm, nm}, cs2 = {CS, CS};
      float e[8];
      for (int i = 0; i < 8; i += 2) {
        const f32x2 sv = {S[k >> 1][e0 + i], S[k >> 1][e0 + i + 1]};
        const f32x2 z = __builtin_elementwise_fma(sv, cs2, nm2);
        e[i] = __builtin_amdgcn_exp2f(z[0]); e[i + 1] = __builtin_amdgcn_exp2f(z[1]);
        const f32x2 ev = {e[i], e[i + 1]};
        sum += ev;
      }
      const u32x4 pk = {pk2(e[0], e[1]), pk2(e[2], e[3]), pk2(e[4], e[5]), pk2(e[6], e[7])};
      pf[k] = __builtin_bit_cast(s16x8, pk);
    };
    if constexpr (g == 17) expo(IC<0>{});
    run_step<ROLE, t, M0, M1, L1>(c,
      [&](auto N) {
        constexpr int n = decltype(N)::value, ks = (n >> 1) & 3;
        if constexpr (n < 128) f[n % NF] = lds_rd(ad[ks] + (n & 1) * 4096);
        else f[n % NF] = lds_tr(ad[n & 1] + ks * 2048, ad[n & 1] + ks * 2048 + 1024);
      },
      [&](auto N) {
        constexpr int n = decltype(N)::value, ks = (n >> 1) & 3, q = (n >> 3) & 3;
        if constexpr (n < 128) {
          constexpr int ti = 2 * q + (n & 1);
          if constexpr (n < 32 && ks == 0) S[ti] = mfma(f[n % NF], qf[ks], f32x16{});
          else S[ti] = mfma(f[n % NF], qf[ks], S[ti]);
          if constexpr (n >= 104) {
            constexpr int tp = 2 * (q - 1) + (ks >> 1), e0 = (n & 3) * 4;
            float m = n == 104 ? S[0][0] : mx;
            for (int e = 0; e < 4; ++e) m = fmaxf(m, S[tp][e0 + e]);
            mx = m;
          }
        } else {
          if constexpr (q == 0 && ks == 0) O[n & 1] = mfma(f[n % NF], pf[4 * q + ks], f32x16{});
          else O[n & 1] = mfma(f[n % NF], pf[4 * q + ks], O[n & 1]);
          if constexpr (n < 160 && (n & 1) == 1 && 4 * q + ks + 1 < 16) expo(IC<4 * q + ks + 1>{});
        }
      });
    if constexpr (g == 16) {
      float m1 = S[7][0];
      for (int e = 0; e < 16; ++e) { mx = fmaxf(mx, S[6][e]); m1 = fmaxf(m1, S[7][e]); }
      mx = half_max(fmaxf(mx, m1));
      if (lane < 32) *(LDSA float*)(size_t)(lds + xs + (wj * 32 + r) * 8 + kh * 4) = mx;
    }
    if constexpr (g > 16 && (g & 3) == 0) {
      constexpr u32 o0 = slot_of(XO, (g - 20) >> 2, 0), o1 = slot_of(XO, (g - 20) >> 2, 1);
      const u32 dst = xch + (o0 + kh * (o1 - o0)) + wj * 4096;
      lds_wr(dst, pack8<0>(O[0])); lds_wr(dst + 1024, pack8<1>(O[0]));
      lds_wr(dst + 2048, pack8<0>(O[1])); lds_wr(dst + 3072, pack8<1>(O[1]));
      if constexpr (g == 20) {
        const float l = half_sum(sum[0] + sum[1]);
        if (lane < 32) *(LDSA float*)(size_t)(lds + xs + 512 + (wj * 32 + r) * 8 + kh * 4) = l;
      }
    }
    issue_rest<ROLE, t, M1 - M0>(c);
  });

  STEP_BARRIER(ST_LAST);
  for (int it = 0; it < SPIN_MAX; ++it) {
    const u32 v = __builtin_amdgcn_raw_buffer_load_b32(rf, r * 4, h * 256, 16);
    if (__builtin_amdgcn_ballot_w64(v != nonce) == 0ull) break;
    asm volatile("" ::: "memory");
  }
  STAMP(NSTEP + 1);
  asm volatile("s_barrier" ::: "memory");
  const int tix = w * 64 + lane;
  if (tix < 224) {
    const rsrc_t rp = __builtin_amdgcn_make_buffer_rsrc(ws + (size_t)(b * 16 + h) * PART_BYTES, 0, PART_BYTES, 0x00020000);
    u32x4 pv[16];
    for (int p = 0; p < 16; ++p) pv[p] = __builtin_amdgcn_raw_buffer_load_b128(rp, tix * 16, p * 3584, 16);
    float a[8];
    for (int k = 0; k < 8; ++k) a[k] = 0.f;
    for (int p = 0; p < 16; ++p)
      for (int k = 0; k < 4; ++k) { a[2 * k] += __uint_as_float(pv[p][k] << 16); a[2 * k + 1] += __uint_as_float(pv[p][k] & 0xffff0000u); }
    const u32x4 o = {pk2(a[0], a[1]), pk2(a[2], a[3]), pk2(a[4], a[5]), pk2(a[6], a[7])};
    *(u32x4*)(out + (size_t)(b * 64 + 4 * h + (tix & 3)) * 896 + (tix >> 2) * 16) = o;
  }
  STAMP(NSTEP + 2);
  STAMP_OUT(0);
  asm volatile(".Lgqa_end_a:");
}

constexpr int wfirst(int g) { return 28 * (g >> 2) + 8 * (g & 3); }

DI void role_proj(const Ctx& c, u32 lds, int w, int lane, int b, int h, char* ws, rsrc_t rf, u32 hold, u32 stv) {
  constexpr int ROLE = 1;
  const int r = lane & 31, hf = lane >> 5, wi = w & 1, wj = w >> 1;
  const u32 rowrd = lds + (wj * 32 + r) * 128 + ((hf ^ ((r >> 1) & 7)) << 4);
  const int blk = (lane >> 4) & 1, q4 = (lane & 15) >> 2, p4 = lane & 3;
  const u32 trc = (((4 * wi + 2 * blk + (p4 & 1)) ^ (((q4 >> 1) & 1) << 2)) << 4) + 8 * (p4 >> 1);
  const u32 xch = lds + lane * 16;
  s16x8 f[NF];

  {
    Base3 trq;
    trq.set(lds + (8 * hf + q4) * 128 + trc);
    s16x8 xf[28];
    f32x16 Q;
    sfor<0, R_WQ + 17>([&](auto T) {
      constexpr int t = decltype(T)::value, g = t - R_WQ;
      STEP_BARRIER(t);
      if constexpr (t == 0) asm volatile("" ::"v"(hold));
      if constexpr (g >= 0) {
        constexpr int M0 = g > 0 ? wfirst(g - 1) : 0, M1 = wfirst(g), L1 = g < 16 ? wfirst(g + 1) : M1;
        run_step<ROLE, t, M0, M1, L1>(c,
          [&](auto N) { constexpr int n = decltype(N)::value; constexpr u32 sb = slot_of(KWQ, n / 28, (n % 28) >> 2) + (n & 3) * 2048;
            f[n % NF] = lds_tr(trq.at<sb>(), trq.at<sb + 512>()); },
          [&](auto N) { constexpr int n = decltype(N)::value;
            if constexpr (n % 28 == 0) Q = mfma(f[n % NF], xf[0], f32x16{});
            else Q = mfma(f[n % NF], xf[n % 28], Q); });
        if constexpr (g > 0 && (g & 3) == 0) {
          constexpr u32 xq = slot_of(XQ, (g >> 2) - 1, 0);
          lds_wr(xch + xq + (wj * 4 + wi * 2) * 1024, pack8<0>(Q));
          lds_wr(xch + xq + (wj * 4 + wi * 2 + 1) * 1024, pack8<1>(Q));
        }
        issue_rest<ROLE, t, M1 - M0>(c);
      }
      if constexpr (t <= 1) {
        sfor<(t == 0 ? 0 : 16), (t == 0 ? 16 : 28)>([&](auto I) {
          constexpr int i = decltype(I)::value;
          constexpr u32 sb = slot_of(KX, 0, i >> 2);
          xf[i] = lds_rd((rowrd ^ (u32)((i & 3) << 5)) + sb);
        });
      }
    });
  }

  u32 warm, req;
  const u32 creq = (8 * wj + (lane & 7)) * 256;
  sfor<R_WQ + 17, R_WO>([&](auto T) {
    constexpr int t = decltype(T)::value;
    STEP_BARRIER(t);
    if constexpr (t == R_WQ + 17) {
      asm volatile("buffer_load_dword %0, %1, %2, %3 offen" : "=v"(warm) : "v"((w * 56 + (lane < 56 ? lane : 55)) * 64), "s"(c.rwo), "s"(b * 14336));
      asm volatile("buffer_load_dword %0, %1, %2, 0 offen offset:128 sc1" : "=v"(req) : "v"(creq), "s"(rf));
    }
    issue<ROLE, t, 0, 64>(c);
  });

  Base3 tro;
  tro.set(lds + (4 * hf + q4) * 128 + trc);
  f32x16 acc[7];
  s16x8 of[2][4];
  u32x4 oa[4], ob[4];
  float linv = 0.f;
  const rsrc_t rp = __builtin_amdgcn_make_buffer_rsrc(ws + (size_t)b * 16 * PART_BYTES, 0, 16 * PART_BYTES, 0x00020000);
  const u32 vst = (8 * wj + (r >> 2)) * PART_BYTES + hf * 64 + (r & 3) * 16 + wi * 256;
  auto put = [&](auto U) {
    constexpr int u = decltype(U)::value;
    const u32 so = h * 3584 + c.r7[u] * 4;
    __builtin_amdgcn_raw_buffer_store_b128(pack8<0>(acc[u]), rp, vst, so, 16);
    __builtin_amdgcn_raw_buffer_store_b128(pack8<1>(acc[u]), rp, vst + 128, so, 16);
  };
  auto merge = [&](auto S_, auto K) {
    constexpr int k = decltype(K)::value;
    const f32x2 li = {linv, linv};
    u32x4 o;
    for (int d = 0; d < 4; ++d) {
      const f32x2 x = {__uint_as_float(oa[k][d] << 16), __uint_as_float(oa[k][d] & 0xffff0000u)};
      const f32x2 y = {__uint_as_float(ob[k][d] << 16), __uint_as_float(ob[k][d] & 0xffff0000u)};
      const f32x2 z = (x + y) * li;
      o[d] = pk2(z[0], z[1]);
    }
    of[decltype(S_)::value & 1][k] = __builtin_bit_cast(s16x8, o);
  };
  sfor<R_WO, NSTEP>([&](auto T) {
    constexpr int t = decltype(T)::value, g = t - R_WO;
    constexpr int M0 = g > 0 ? wfirst(g - 1) : 0, M1 = wfirst(g), L1 = wfirst(g + 1);
    STEP_BARRIER(t);
    if constexpr (g == 0) {
      static_assert(PL.vm[1][R_WO] <= 2 * (PL.first[R_WO + 1] - PL.first[R_WQ + 18]), "the wait above must cover the two register loads");
      asm volatile("" : "+v"(req), "+v"(warm));
    }
    if constexpr ((g & 3) == 0) {
      if constexpr (g == 0) {
        constexpr u32 xs = slot_of(XS, 0, 0);
        const f32x2 ss = *(const LDSA f32x2*)(size_t)(lds + xs + 512 + (wj * 32 + r) * 8);
        linv = 1.0f / (ss[0] + ss[1]);
      }
      constexpr u32 o0 = slot_of(XO, g >> 2, 0), o1 = slot_of(XO, g >> 2, 1);
      sfor<0, 4>([&](auto I) { constexpr int i = decltype(I)::value;
        oa[i] = __builtin_bit_cast(u32x4, lds_rd(xch + o0 + (wj * 4 + i) * 1024));
        ob[i] = __builtin_bit_cast(u32x4, lds_rd(xch + o1 + (wj * 4 + i) * 1024)); });
      SB();
    }
    auto ld = [&](auto N) { constexpr int n = decltype(N)::value; constexpr u32 sb = slot_of(KWO, n / 28, (n % 28) >> 2) + (n & 3) * 2048;
      f[n % NF] = lds_tr(tro.at<sb>(), tro.at<sb + 1024>()); };
    auto mm = [&](auto N) { constexpr int n = decltype(N)::value, u = (n % 28) >> 2, ks = n & 3;
      if constexpr (n < 28 && ks == 0) acc[u] = mfma(f[n % NF], of[0][ks], f32x16{});
      else acc[u] = mfma(f[n % NF], of[(n / 28) & 1][ks], acc[u]);
      if constexpr (u == 6 && n < 84) merge(IC<n / 28 + 1>{}, IC<ks>{});
    };
    run_step<ROLE, t, M0, M1, L1>(c, ld, mm);
    if constexpr (g == 0) sfor<0, 4>([&](auto K) { merge(IC<0>{}, K); });
    issue_rest<ROLE, t, M1 - M0>(c);
    if constexpr (t == ST_LAST) {
      sfor<L1, M1 + NF>(ld);
      SB();
      sfor<M1, 112>([&](auto N) { constexpr int n = decltype(N)::value, m = n - M1;
        mm(N);
        if constexpr (n + NF < 112) ld(IC<n + NF>{});
        if constexpr (m >= 5 && (m & 3) == 1) put(IC<(m >> 2) - 1>{});
        SB();
      });
    }
  });
  put(IC<6>{});

  STAMP(NSTEP + 1);
  asm volatile("s_waitcnt vmcnt(0)" ::: "memory");
  STAMP(NSTEP + 2);
  const u32 cin = creq + (h * 2 + wi) * 4;
  if (lane < 8) __builtin_amdgcn_raw_buffer_store_b32(req, rf, cin, 0, 16);
  asm volatile("s_barrier" ::: "memory");
  const u32 now = __builtin_amdgcn_raw_buffer_load_b32(rf, creq + 128, 0, 16);
  if (lane < 8 && now != req) __builtin_amdgcn_raw_buffer_store_b32(now, rf, cin, 0, 16);
  STAMP(NSTEP + 3);
  STAMP_OUT(1);
  asm volatile(".Lgqa_end_p:");
}

__global__ __launch_bounds__(512, 2) void gqa_block_kernel(const char* __restrict__ x, const char* __restrict__ k, const char* __restrict__ v,
                                                           const char* __restrict__ wq, const char* __restrict__ wo, char* out, char* ws) {
  __shared__ __attribute__((aligned(16))) char smem[LDS_BYTES];
  u32 stv = 0;
  STAMP(0);
  const u32 lds = (u32)(size_t)(LDSA char*)smem;
  const int bid = blockIdx.x, kvh = bid & 7, b = bid >> 4, hh = (bid >> 3) & 1, h = kvh * 2 + hh;
  const int wid = __builtin_amdgcn_readfirstlane(threadIdx.x >> 6), lane = threadIdx.x & 63;

  Ctx c;
  c.rx = __builtin_amdgcn_make_buffer_rsrc((void*)(x + (size_t)b * 57344), 0, 57344, 0x00020000);
  c.rk = __builtin_amdgcn_make_buffer_rsrc((void*)(k + (size_t)(b * 8 + kvh) * 262144), 0, 262144, 0x00020000);
  c.rv = __builtin_amdgcn_make_buffer_rsrc((void*)(v + (size_t)(b * 8 + kvh) * 262144), 0, 262144, 0x00020000);
  c.rwq = __builtin_amdgcn_make_buffer_rsrc((void*)(wq + h * 512), 0, 3670016 - h * 512, 0x00020000);
  c.rwo = __builtin_amdgcn_make_buffer_rsrc((void*)(wo + (size_t)h * 229376), 0, 229376, 0x00020000);
  const u32 row = (wid & 3) * 8 + (lane >> 3);
  const u32 crow = ((lane & 7) ^ ((row >> 1) & 7)) << 4;
  const u32 ctr = ((lane & 7) ^ (((row >> 1) & 1) << 2)) << 4;
  c.vx = row * 896 + crow; c.vk = row * 512 + crow;
  c.vwq = row * 8192 + ctr; c.vv = row * 512 + ctr; c.vwo = row * 896 + ctr;
  c.m0base = lds + (wid & 3) * 1024;
  for (int s = 0; s < 4; ++s) { c.sl[s] = ((s + b) & 3) * 128; c.kvs[s] = c.sl[s] | (hh << 17); }
  const int wr = (b >> 2) * 2 + hh * 3;
  for (int i = 0; i < 7; ++i) { const int t = i + wr; c.r7[i] = (t >= 14 ? t - 14 : t >= 7 ? t - 7 : t) * 128; }

  const rsrc_t rf = __builtin_amdgcn_make_buffer_rsrc(ws + WS_FLAGS + b * 4096, 0, 4096, 0x00020000);
  u32 inbox = 0;
  if (wid == 7) {
    u64 pc;
    u32 len;
    asm volatile("s_getpc_b64 %0\n.Lgqa_pc:\n\ts_mov_b32 %1, .Lgqa_end_a-.Lgqa_pc\n\ts_max_i32 %1, %1, .Lgqa_end_p-.Lgqa_pc" : "=s"(pc), "=s"(len)::"scc");
    const rsrc_t rc = __builtin_amdgcn_make_buffer_rsrc((void*)pc, 0, len, 0x00020000);
    asm volatile("buffer_load_dword %0, %1, %2, 0 offen" : "=v"(inbox) : "v"(((lane & 15) * 32 + (bid >> 3)) * 64), "s"(rc));
  }
  if (wid == 0) asm volatile("buffer_load_dword %0, %1, %2, %3 offen sc1" : "=v"(inbox) : "v"((lane & 31) * 4), "s"(rf), "s"(h * 256));
  if (wid < 4) role_attn(c, lds, wid, lane, b, h, ws, out, rf, inbox, stv);
  else role_proj(c, lds, wid - 4, lane, b, h, ws, rf, inbox, stv);
}

}

extern "C" void kernel_launch(void* const* d_in, const int* in_sizes, int n_in, void* d_out, int out_size, void* d_ws, size_t ws_size,
                              hipStream_t stream) {
  gqa_block_kernel<<<256, 512, 0, stream>>>((const char*)d_in[0], (const char*)d_in[1], (const char*)d_in[2], (const char*)d_in[3],
                                            (const char*)d_in[4], (char*)d_out, (char*)d_ws);
}
```

```cpp
#include <hip/hip_runtime.h>

namespace {

typedef unsigned int u32;
typedef unsigned long long u64;
typedef short s16x4 __attribute__((ext_vector_type(4)));
typedef short s16x8 __attribute__((ext_vector_type(8)));
typedef float f32x2 __attribute__((ext_vector_type(2)));
typedef float f32x4 __attribute__((ext_vector_type(4)));
typedef __bf16 bf16x2 __attribute__((ext_vector_type(2)));
typedef u32 u32x2 __attribute__((ext_vector_type(2)));
typedef u32 u32x4 __attribute__((ext_vector_type(4)));
#define LDSA __attribute__((address_space(3)))
#define DI __device__ __forceinline__
typedef __amdgpu_buffer_rsrc_t rsrc_t;

constexpr int NSLOT = 20;
constexpr int LDS_BYTES = NSLOT * 8192;
constexpr u32 PART_BYTES = 57344;
constexpr u32 WS_FLAGS = 16u * 16u * PART_BYTES;
constexpr u32 BOX = 256, REQ = 128;
constexpr int SPIN_MAX = 1 << 20;

enum { KX = 0, KWQ = 1, KK = 2, KV = 3, KWO = 4 };
constexpr int NC = 120, NLD = 127, MAXST = 64;
constexpr int DEPTH = 6;
constexpr int SLICE_TICKS = 186;
constexpr int STAGGER = 12;

struct Plan {
  int ckind[NC], cs[NC], ci[NC], cstep[NC];
  int nstep, cfirst[MAXST + 1];
  int kind[NLD], s[NLD], i[NLD], at[NLD], use[NLD], phys[NLD];
  int first[MAXST + 2];
  int extra[NLD];
  int vm[MAXST];
  bool ok;
};

constexpr Plan make_plan() {
  Plan p{};
  int nc = 0, nl = 0, nw = 0;
  auto more = [&](int kind, int s, int i0, int cnt) {
    for (int c = 0; c < cnt; ++c) { p.ckind[nc] = kind; p.cs[nc] = s; p.ci[nc] = i0 + c; p.cstep[nc] = p.nstep - 1; ++nc; }
  };
  auto use = [&](int kind, int s, int i0, int cnt) { p.cfirst[p.nstep] = nc; p.first[p.nstep + 1] = nl; ++p.nstep; more(kind, s, i0, cnt); };
  auto load = [&](int kind, int s, int i0, int cnt) { for (int c = 0; c < cnt; ++c) { p.kind[nl] = kind; p.s[nl] = s; p.i[nl] = i0 + c; p.at[nl] = p.nstep - 1; ++nl; } };
  auto wo = [&](int cnt) { for (int c = 0; c < cnt && nw < 28; ++c, ++nw) load(KWO, nw & 3, nw >> 2, 1); };
  load(KX, 0, 0, 2); load(KWQ, 0, 0, 2); load(KX, 0, 2, 2); load(KWQ, 0, 2, 2); load(KX, 0, 4, 2);
  use(KWQ, 0, 0, 2); load(KX, 0, 6, 1); load(KWQ, 0, 4, 3);
  use(KWQ, 0, 2, 2); load(KK, 0, 0, 6);
  use(KWQ, 0, 4, 3); load(KK, 0, 6, 2); load(KWQ, 1, 0, 2);
  use(KK, 0, 0, 4); load(KWQ, 1, 2, 5);
  use(KK, 0, 4, 4); load(KK, 1, 0, 4);
  use(KWQ, 1, 0, 4); load(KK, 1, 4, 2);
  use(KWQ, 1, 4, 3); load(KK, 1, 6, 2); load(KWQ, 2, 0, 5);
  use(KK, 1, 0, 4); load(KWQ, 2, 5, 2); load(KK, 2, 0, 4);
  use(KK, 1, 4, 4); load(KK, 2, 4, 2);
  use(KWQ, 2, 0, 4); load(KK, 2, 6, 2); load(KWQ, 3, 0, 2);
  use(KWQ, 2, 4, 3); load(KWQ, 3, 2, 5);
  use(KK, 2, 0, 4); load(KK, 3, 0, 4);
  use(KK, 2, 4, 4); load(KK, 3, 4, 2);
  use(KWQ, 3, 0, 4); load(KK, 3, 6, 2); load(KV, 0, 0, 4);
  use(KWQ, 3, 4, 3); load(KV, 0, 4, 1);
  use(KK, 3, 0, 4);
  use(KK, 3, 4, 4); load(KV, 0, 5, 3); load(KV, 1, 0, 7);
  use(KV, 0, 0, 4); load(KV, 1, 7, 1); load(KV, 2, 0, 3);
  use(KV, 0, 4, 4); load(KV, 2, 3, 3);
  use(KV, 1, 0, 4);
  use(KV, 1, 4, 4); load(KV, 2, 6, 2); load(KV, 3, 0, 4);
  use(KV, 2, 0, 4); load(KV, 3, 4, 2);
  use(KV, 2, 4, 4); load(KV, 3, 6, 2); wo(2);
  use(KV, 3, 0, 4); wo(5);
  use(KV, 3, 4, 4); wo(4);
  for (int u = 0; u < 7; ++u) {
    if (u < 6) use(KWO, 0, u, 1); else more(KWO, 0, u, 1);
    more(KWO, 1, u, 1); more(KWO, 2, u, 1); more(KWO, 3, u, 1);
    if (u < 6) wo(4);
  }
  p.cfirst[p.nstep] = nc;
  p.first[p.nstep + 1] = nl;
  p.ok = nc == NC && nl == NLD;
  for (int e = 0; e < NLD; ++e) {
    p.use[e] = -1;
    for (int c = 0; c < NC; ++c)
      if (p.ckind[c] == (p.kind[e] == KX ? KWQ : p.kind[e]) && p.cs[c] == p.s[e] && p.ci[c] == p.i[e]) p.use[e] = c;
    if (p.use[e] < 0 || p.at[e] >= p.cstep[p.use[e]]) p.ok = false;
    if (p.kind[e] == KV && p.s[e] < 2 && p.i[e] == 1) p.extra[e] = 1;
    if ((p.kind[e] == KK || p.kind[e] == KV) && p.s[e] == 2 && p.i[e] == 0) p.extra[e] = 2;
    if (p.kind[e] == KV && p.s[e] == 0 && p.i[e] == 0) p.extra[e] = 2;
  }
  int owner[NSLOT] = {}, pos = 0;
  for (int k = 0; k < NSLOT; ++k) owner[k] = -1;
  for (int t = -1; t < p.nstep; ++t) {
    for (int k = 0; k < NSLOT; ++k)
      if (owner[k] >= 0 && t >= 0 && p.use[owner[k]] <= p.cfirst[t] - 2) owner[k] = -1;
    for (int e = p.first[t + 1]; e < p.first[t + 2]; ++e) {
      int tries = 0;
      while (owner[pos] >= 0 && tries < NSLOT) { pos = (pos + 1) % NSLOT; ++tries; }
      if (owner[pos] >= 0) p.ok = false;
      owner[pos] = e; p.phys[e] = pos;
    }
  }
  for (int t = 0; t < p.nstep; ++t) {
    int last = -1;
    for (int e = 0; e < NLD; ++e) if (p.cstep[p.use[e]] <= t) last = e;
    p.vm[t] = 2 * (p.first[t + 1] - 1 - last);
    for (int e = last; e < p.first[t + 1]; ++e) if (p.extra[e]) ++p.vm[t];
  }
  return p;
}
constexpr Plan PL = make_plan();
static_assert(PL.ok, "infeasible schedule");
static_assert(DEPTH <= 8, "at a barrier only the last tile may have reads in flight");
constexpr int NSTEP = PL.nstep;
constexpr int slot_of(int kind, int s, int i) {
  for (int e = 0; e < NLD; ++e) if (PL.kind[e] == kind && PL.s[e] == s && PL.i[e] == i) return PL.phys[e] * 8192;
  return -1;
}
constexpr int cslot(int c) { return slot_of(PL.ckind[c], PL.cs[c], PL.ci[c]); }
constexpr int aclass(int kind) { return kind == KK ? 0 : kind == KWQ ? 1 : 2; }
constexpr int prev_window(int c) {
  for (int d = c - 1; d >= 0; --d) if (aclass(PL.ckind[d]) == aclass(PL.ckind[c])) return cslot(d) >> 16;
  return 0;
}
constexpr int use_pos(int kind, int s, int i) {
  for (int c = 0; c < NC; ++c) if (PL.ckind[c] == kind && PL.cs[c] == s && PL.ci[c] == i) return c;
  return -1;
}
constexpr int floor_at(int t) {
  const int c = PL.cfirst[t], k = PL.ckind[c], sl = PL.cs[c], i = PL.ci[c];
  if (k == KK && i == 0) return sl * SLICE_TICKS;
  if (k == KWQ && sl > 0 && i == 4) return (sl - 1) * SLICE_TICKS + SLICE_TICKS * 11 / 16;
  return 0;
}

template <int I> struct IC { static constexpr int value = I; };
template <int A, int B, class F> DI void sfor(F&& f) {
  if constexpr (B - A == 1) f(IC<A>{});
  else if constexpr (B - A > 1) { sfor<A, A + (B - A) / 2>(f); sfor<A + (B - A) / 2, B>(f); }
}

#define SB() __builtin_amdgcn_sched_barrier(0)
template <bool FIRST> DI void mfma(f32x4& c, s16x8 a, u32x4 b) {
  if constexpr (FIRST) asm volatile("v_mfma_f32_16x16x32_bf16 %0, %1, %2, 0" : "=v"(c) : "v"(a), "v"(b));
  else asm volatile("v_mfma_f32_16x16x32_bf16 %0, %1, %2, %0" : "+v"(c) : "v"(a), "v"(b));
}
DI void settle(f32x4& a, f32x4& b, f32x4& c, f32x4& d) { asm volatile("s_nop 7" : "+v"(a), "+v"(b), "+v"(c), "+v"(d)); }
DI void ready(u32x4& a, u32x4& b) { asm volatile("s_nop 1" : "+v"(a), "+v"(b)); }
DI float max3(float a, float b, float c) { float r; asm("v_max3_f32 %0, %1, %2, %3" : "=v"(r) : "v"(a), "v"(b), "v"(c)); return r; }
DI float max5(float m, f32x4 v) {
  float r;
  asm("v_max3_f32 %0, %1, %2, %3\n\tv_max3_f32 %0, %0, %4, %5" : "=&v"(r) : "v"(m), "v"(v[0]), "v"(v[1]), "v"(v[2]), "v"(v[3]));
  return r;
}
DI u32 scale_pack(float a, float b, float f) {
  u32 r;
  float t;
  asm("v_mul_f32 %0, %2, %4\n\tv_mul_f32 %1, %3, %4\n\tv_cvt_pk_bf16_f32 %0, %0, %1" : "=&v"(r), "=&v"(t) : "v"(a), "v"(b), "v"(f));
  return r;
}
DI u32 pk2(float lo, float hi) { f32x2 v = {lo, hi}; return __builtin_bit_cast(u32, __builtin_convertvector(v, bf16x2)); }
DI u32x4 pack2(f32x4 a, f32x4 b) { u32x4 r = {pk2(a[0], a[1]), pk2(a[2], a[3]), pk2(b[0], b[1]), pk2(b[2], b[3])}; return r; }
DI u32x4 pack2_natural(f32x4 a, f32x4 b) {
  const u32x4 v = pack2(a, b);
  u32x4 r;
  for (int d = 0; d < 2; ++d) {
    const auto x = __builtin_amdgcn_permlane32_swap(v[d], v[2 + d], false, false);
    const auto y = __builtin_amdgcn_permlane16_swap(x[0], x[1], false, false);
    r[d] = y[0]; r[2 + d] = y[1];
  }
  return r;
}
DI float col_max(float v) {
  const auto x = __builtin_amdgcn_permlane32_swap(__float_as_uint(v), __float_as_uint(v), false, false);
  v = max3(__uint_as_float(x[0]), __uint_as_float(x[1]), __uint_as_float(x[1]));
  const auto y = __builtin_amdgcn_permlane16_swap(__float_as_uint(v), __float_as_uint(v), false, false);
  return max3(__uint_as_float(y[0]), __uint_as_float(y[1]), __uint_as_float(y[1]));
}
DI s16x8 lds_rd(u32 a) { return *(const LDSA s16x8*)(size_t)a; }
DI s16x8 lds_tr(u32 a0, u32 a1) {
  const s16x4 lo = __builtin_amdgcn_ds_read_tr16_b64_v4i16((LDSA s16x4*)(size_t)a0);
  const s16x4 hi = __builtin_amdgcn_ds_read_tr16_b64_v4i16((LDSA s16x4*)(size_t)a1);
  return __builtin_shufflevector(lo, hi, 0, 1, 2, 3, 4, 5, 6, 7);
}
DI void ld_dev(u32& dst, u32 voff, rsrc_t r, u32 soff) { asm volatile("buffer_load_dword %0, %1, %2, %3 offen sc1" : "+v"(dst) : "v"(voff), "s"(r), "s"(soff)); }

DI void role_compute(u32 lds, int w, int lane, int b, int h, u32 rot, char* ws, rsrc_t rf) {
  const int r = lane & 15, g = lane >> 4, q4 = r >> 2, p4 = lane & 3;
  const u32 swz = ((p4 >> 1) ^ ((((q4 >> 1) & 1) | ((g & 1) << 1)) << 1)) << 4;
  u32 tb[4], rb[2];
  for (int c = 0; c < 4; ++c) tb[c] = (lds + (8 * g + q4) * 128 + swz + 8 * (p4 & 1)) ^ (u32)(c << 5);
  for (int s = 0; s < 2; ++s) rb[s] = (lds + r * 128 + ((g ^ ((r >> 1) & 7)) << 4)) ^ (u32)(s << 6);
  const u32 xrow = w * 2048;
  __builtin_amdgcn_s_setprio(1);

  s16x8 f[DEPTH];
  u32x4 xf[14], qf[2], pf[16], of[4][2];
  f32x4 T[4], O[2][4];
  f32x4 S[32], acc[28], lsum;
  s16x8 ones = {0x3f80, 0x3f80, 0x3f80, 0x3f80, 0x3f80, 0x3f80, 0x3f80, 0x3f80};
  float mx = 0.f, nm = 0.f, linv = 0.f;
  u32 req = 0;
  constexpr float CS = 0.0625f * 1.4426950408889634f;
  constexpr int C_V0 = use_pos(KV, 0, 0), C_K3 = use_pos(KK, 3, 0), NV0 = 8 * C_V0;
  const u32 creq = (4 * w + (lane & 3)) * BOX;
  const rsrc_t rp = __builtin_amdgcn_make_buffer_rsrc(ws + (size_t)b * 16 * PART_BYTES, 0, 16 * PART_BYTES, 0x00020000);
  const u32 vst = (4 * w + q4) * PART_BYTES + g * 64 + p4 * 16;
  const u32 cin = lane < 4 ? creq + h * 4 : 0x10000000u;

  auto rd = [&](auto N) {
    constexpr int n = decltype(N)::value, c = n >> 3, m = n & 7, kind = PL.ckind[c];
    constexpr u32 sb = cslot(c), off = sb & 65535u;
    constexpr int dw = (int)(sb >> 16) - prev_window(c);
    if constexpr (m == 0 && PL.cfirst[PL.cstep[c]] == c) asm volatile("s_barrier" ::: "memory");
    if constexpr (c == C_V0 && m == 0) {
      for (int cb = 0; cb < 4; ++cb) tb[cb] = (lds + (4 * g + q4) * 128 + swz + 8 * (p4 & 1)) ^ (u32)(cb << 5);
    }
    if constexpr (kind == KK) {
      if constexpr (dw != 0 && (m & 3) == 0) { rb[m >> 2] += dw * 65536; asm volatile("" : "+v"(rb[m >> 2])); }
      f[n % DEPTH] = lds_rd(rb[m >> 2] + off + (m & 3) * 2048);
    } else {
      if constexpr (dw != 0 && m < 4) { tb[m] += dw * 65536; asm volatile("" : "+v"(tb[m])); }
      if constexpr (kind == KWQ && PL.cs[c] == 0 && (m & 3) == 0) {
        constexpr u32 xs = slot_of(KX, 0, PL.ci[c]);
        xf[2 * PL.ci[c] + (m >> 2)] = __builtin_bit_cast(u32x4, lds_rd(rb[m >> 2] + xrow + xs));
      }
      constexpr u32 a = off + (m >> 2) * 4096;
      f[n % DEPTH] = lds_tr(tb[m & 3] + a, tb[m & 3] + a + (kind == KWQ ? 512 : 2048));
    }
  };
  auto expo = [&](auto K) {
    constexpr int k = decltype(K)::value;
    const f32x2 nm2 = {nm, nm}, cs2 = {CS, CS};
    f32x2 z[4];
    for (int i = 0; i < 4; ++i) { const f32x2 sv = {S[2 * k + (i >> 1)][2 * (i & 1)], S[2 * k + (i >> 1)][2 * (i & 1) + 1]}; z[i] = __builtin_elementwise_fma(sv, cs2, nm2); }
    SB();
    if constexpr (k > 0) mfma<k == 1>(lsum, ones, pf[k - 1]);
    SB();
    for (int i = 0; i < 4; ++i) { z[i][0] = __builtin_amdgcn_exp2f(z[i][0]); z[i][1] = __builtin_amdgcn_exp2f(z[i][1]); }
    SB();
    for (int i = 0; i < 4; ++i) pf[k][i] = pk2(z[i][0], z[i][1]);
    SB();
  };
  auto put = [&](auto U, auto H) {
    constexpr int u = decltype(U)::value, hh = decltype(H)::value;
    const u32 nb = (rot >> (4 * u)) & 7;
    __builtin_amdgcn_raw_buffer_store_b128(pack2(acc[4 * u + 2 * hh], acc[4 * u + 2 * hh + 1]), rp, vst + hh * 256, h * 3584 + nb * 512, 16);
  };

  sfor<0, DEPTH>(rd);
  SB();
  asm volatile("" ::"v"(f[0]), "v"(xf[0]));
  sfor<0, 8 * NC>([&](auto N) {
    constexpr int n = decltype(N)::value, c = n >> 3, m = n & 7, kind = PL.ckind[c], s = PL.cs[c], i = PL.ci[c], s2 = m >> 2, cb = m & 3;
    constexpr int cp = c > 0 ? c - 1 : 0;
    if constexpr (n == NV0) {
      settle(S[28], S[29], S[30], S[31]);
      mx = max3(mx, max5(max5(S[28][0], S[28]), S[29]), max5(max5(S[30][0], S[30]), S[31]));
      nm = -CS * col_max(mx);
      asm volatile("" : "+v"(ones));
      sfor<0, 14>(expo);
      SB();
      sfor<NV0, NV0 + DEPTH>(rd);
      SB();
      sfor<14, 16>(expo);
      ready(pf[14], pf[15]);
      mfma<false>(lsum, ones, pf[15]);
      asm volatile("s_waitcnt vmcnt(0)" : "+v"(req));
      SB();
    }
    if constexpr (kind == KWQ) {
      mfma<i == 0 && s2 == 0>(T[cb], f[n % DEPTH], xf[2 * i + s2]);
    } else if constexpr (kind == KK) {
      if constexpr (i == 0 && m == 0) {
        settle(T[0], T[1], T[2], T[3]);
        qf[0] = pack2_natural(T[0], T[1]); qf[1] = pack2_natural(T[2], T[3]);
        ready(qf[0], qf[1]);
      }
      mfma<s == 0 && s2 == 0>(S[4 * i + cb], f[n % DEPTH], qf[s2]);
    } else if constexpr (kind == KV) {
      mfma<i == 0 && s2 == 0>(O[s & 1][cb], f[n % DEPTH], pf[2 * i + s2]);
    } else {
      mfma<s == 0 && s2 == 0>(acc[4 * i + cb], f[n % DEPTH], of[s][s2]);
    }
    if constexpr (n + 1 < 8 * NC && n + 1 != NV0) asm volatile("" ::"v"(f[(n + 1) % DEPTH]));
    SB();
    if constexpr (n + DEPTH < 8 * NC && (n >= NV0 || n + DEPTH < NV0)) rd(IC<n + DEPTH>{});
    SB();
    if constexpr (kind == KK && s == 3 && i > 0 && (m & 1) == 1) {
      constexpr int t = 4 * (i - 1) + (m >> 1);
      if constexpr (n + DEPTH >= NV0) asm volatile("s_nop 3");
      mx = max5(t == 0 ? S[0][0] : mx, S[t]);
    }
    if constexpr (n == NV0 + 8) linv = __builtin_amdgcn_rcpf(lsum[0]);
    if constexpr (PL.ckind[cp] == KV && PL.ci[cp] == 7) {
      constexpr int sp = PL.cs[cp];
      static_assert(kind != KWO || s != sp, "the slice is not ready yet");
      of[sp][m >> 2][m & 3] = scale_pack(O[sp & 1][m >> 1][2 * (m & 1)], O[sp & 1][m >> 1][2 * (m & 1) + 1], linv);
    }
    if constexpr (PL.ckind[cp] == KWO && PL.cs[cp] == 3 && (m == 2 || m == 5)) put(IC<PL.ci[cp]>{}, IC<(m >> 2)>{});
    if constexpr (n == 8 * C_K3) ld_dev(req, creq, rf, REQ);
    SB();
  });
  settle(acc[24], acc[25], acc[26], acc[27]);
  put(IC<6>{}, IC<0>{});
  put(IC<6>{}, IC<1>{});

  asm volatile("s_barrier" ::: "memory");
  asm volatile("s_waitcnt vmcnt(0)" ::: "memory");
  __builtin_amdgcn_raw_buffer_store_b32(req, rf, cin, 0, 16);
  asm volatile("s_barrier" ::: "memory");
  const u32 now = __builtin_amdgcn_raw_buffer_load_b32(rf, creq + REQ, 0, 16);
  if (now != req) __builtin_amdgcn_raw_buffer_store_b32(now, rf, cin, 0, 16);
  asm volatile(".Lgqa_end_c:");
  __builtin_amdgcn_endpgm();
}

struct Ctx {
  rsrc_t rx, rk, rv, rwq, rwo;
  u32 vx[2], vwq[2], vk[2], vv[2], vwo[2];
  u32 m0base;
  u32 sl[4];
  u32 slo[4];
  u32 kvs[4];
  u32 r7[7], r7q[7];
};
#define DMA2(OP, BC, POL)                                                                                                        \
  asm volatile("s_add_u32 m0, %1, %2\n\t" OP " %0, %6, %7\n\tbuffer_load_dwordx4 %4, %3, %0 offen" POL " lds\n\t"                \
               "s_add_u32 m0, m0, 0x1000\n\ts_nop 0\n\tbuffer_load_dwordx4 %5, %3, %0 offen" POL " lds"                          \
               : "=&s"(tmp) : "s"(m0base), "i"(OFF), "s"(r), "v"(v[0]), "v"(v[1]), "s"(a), BC(bb) : "scc")
template <int OFF> DI void dma_add(u32 m0base, rsrc_t r, const u32* v, u32 a, u32 bb) { u32 tmp; DMA2("s_add_u32", "s", ""); }
template <int OFF, int bb, bool LAST_USE> DI void dma_xor(u32 m0base, rsrc_t r, const u32* v, u32 a) {
  u32 tmp;
  if constexpr (LAST_USE) DMA2("s_xor_b32", "i", " nt"); else DMA2("s_xor_b32", "i", "");
}
template <int E> DI void issue(const Ctx& c) {
  constexpr int kind = PL.kind[E], s = PL.s[E], i = PL.i[E], off = PL.phys[E] * 8192;
  if constexpr (kind == KX) dma_xor<off, 0, false>(c.m0base, c.rx, c.vx, c.r7[i]);
  else if constexpr (kind == KWQ) dma_add<off>(c.m0base, c.rwq, c.vwq, c.sl[s], c.r7q[i]);
  else if constexpr (kind == KK) dma_xor<off, (i << 15), (i >= 4)>(c.m0base, c.rk, c.vk, c.kvs[s]);
  else if constexpr (kind == KV) dma_xor<off, (i << 15), (i >= 4)>(c.m0base, c.rv, c.vv, c.kvs[s]);
  else dma_add<off>(c.m0base, c.rwo, c.vwo, c.slo[s], c.r7[i]);
}

DI void role_load(const Ctx& c, int w, int lane, int b, int h, char* ws, char* out, rsrc_t rf, rsrc_t rc, u32 vcode, u32 vbox) {
  u32 aux, warm, tref = 0;
  asm volatile("buffer_load_dword %0, %2, %4, %6 offen sc1\n\tbuffer_load_dword %1, %3, %5, 0 offen sc1"
               : "=&v"(aux), "=&v"(warm) : "v"(vbox), "v"(vcode), "s"(rf), "s"(rc), "s"(h * BOX));
  sfor<-1, NSTEP>([&](auto T) {
    constexpr int t = decltype(T)::value;
    if constexpr (t >= 0) {
      if constexpr (floor_at(t) > 0) {
        while ((u32)__builtin_amdgcn_s_memrealtime() - tref < (u32)floor_at(t)) __builtin_amdgcn_s_sleep(1);
      }
      asm volatile("s_waitcnt vmcnt(%0)\n\ts_barrier" ::"i"(PL.vm[t]) : "memory");
      if constexpr (PL.cfirst[t] == use_pos(KK, 0, 0)) tref = (u32)__builtin_amdgcn_s_memrealtime();
    }
    if constexpr (t == 0) {
      asm volatile("" : "+v"(aux), "+v"(warm));
      if (w == 0) {
        u32 nonce = __builtin_amdgcn_readfirstlane(aux) + 1u;
        while (__builtin_amdgcn_ballot_w64(aux == nonce) != 0ull) ++nonce;
        if (lane == 0) asm volatile("buffer_store_dword %0, off, %1, %2 sc1" ::"v"(nonce), "s"(rf), "s"(h * BOX + REQ) : "memory");
      }
    }
    if constexpr (t == 1) { if (b >= 8) __builtin_amdgcn_s_sleep(STAGGER); }
    sfor<PL.first[t + 1], PL.first[t + 2]>([&](auto E) {
      constexpr int e = decltype(E)::value;
      issue<e>(c);
      if constexpr (PL.extra[e] == 1) ld_dev(warm, (w * 28 + (lane < 28 ? lane : 27)) * 64, c.rwo, b * 14336 + (PL.s[e] & 1) * 7168);
      if constexpr (PL.extra[e] == 2) ld_dev(warm, vcode, rc, 0);
    });
  });
  asm volatile("s_waitcnt vmcnt(0)" : "+v"(warm)::"memory");

  const int tix = w * 64 + lane;
  u32x4 pv[16];
  {
    const rsrc_t rp = __builtin_amdgcn_make_buffer_rsrc(ws + (size_t)(b * 16 + h) * PART_BYTES, 0, PART_BYTES, 0x00020000);
    u32 val, done = 0, nw, s0, spin = SPIN_MAX;
#define FETCH(P, OFF)                                                                                                            \
  "s_bitcmp1_b32 %[nw], " #P "\n\ts_cbranch_scc0 .Lgqa_p" #P "\n\ts_mov_b32 %[s0], " #OFF "\n\t"                                 \
  "buffer_load_dwordx4 %[p" #P "], %[vo], %[rp], %[s0] offen sc1\n.Lgqa_p" #P ":\n\t"
    asm volatile(
        "s_barrier\n"
        ".Lgqa_poll:\n\t"
        "buffer_load_dword %[val], %[vf], %[rf], %[sf] offen sc1\n\t"
        "s_waitcnt vmcnt(0)\n\t"
        "v_readlane_b32 %[s0], %[val], 16\n\t"
        "s_nop 1\n\t"
        "v_cmp_eq_u32 vcc, %[s0], %[val]\n\t"
        "s_nop 0\n\t"
        "s_and_b32 %[s0], vcc_lo, 0xffff\n\t"
        "s_andn2_b32 %[nw], %[s0], %[done]\n\t"
        "s_cbranch_scc1 .Lgqa_new\n\t"
        "s_sleep 2\n\t"
        "s_sub_u32 %[spin], %[spin], 1\n\t"
        "s_cbranch_scc0 .Lgqa_poll\n\t"
        "s_branch .Lgqa_all\n"
        ".Lgqa_new:\n\t"
        "s_or_b32 %[done], %[done], %[nw]\n\t"
        FETCH(0, 0) FETCH(1, 3584) FETCH(2, 7168) FETCH(3, 10752) FETCH(4, 14336) FETCH(5, 17920) FETCH(6, 21504) FETCH(7, 25088)
        FETCH(8, 28672) FETCH(9, 32256) FETCH(10, 35840) FETCH(11, 39424) FETCH(12, 43008) FETCH(13, 46592) FETCH(14, 50176) FETCH(15, 53760)
        "s_cmp_lg_u32 %[done], 0xffff\n\t"
        "s_cbranch_scc1 .Lgqa_poll\n"
        ".Lgqa_all:\n\t"
        "s_barrier\n\t"
        "s_waitcnt vmcnt(0)"
        : [p0] "=&v"(pv[0]), [p1] "=&v"(pv[1]), [p2] "=&v"(pv[2]), [p3] "=&v"(pv[3]), [p4] "=&v"(pv[4]), [p5] "=&v"(pv[5]), [p6] "=&v"(pv[6]),
          [p7] "=&v"(pv[7]), [p8] "=&v"(pv[8]), [p9] "=&v"(pv[9]), [p10] "=&v"(pv[10]), [p11] "=&v"(pv[11]), [p12] "=&v"(pv[12]),
          [p13] "=&v"(pv[13]), [p14] "=&v"(pv[14]), [p15] "=&v"(pv[15]), [val] "=&v"(val), [done] "+s"(done), [nw] "=&s"(nw), [s0] "=&s"(s0),
          [spin] "+s"(spin)
        : [vf] "v"(lane < 16 ? lane * 4 : REQ), [vo] "v"(tix < 224 ? tix * 16 : 0x10000000), [rf] "s"(rf), [rp] "s"(rp), [sf] "s"(h * BOX)
        : "scc", "vcc", "memory");
#undef FETCH
  }
  if (tix < 224) {
    float a[8];
    for (int e = 0; e < 8; ++e) a[e] = 0.f;
    for (int p = 0; p < 16; ++p)
      for (int e = 0; e < 4; ++e) { a[2 * e] += __uint_as_float(pv[p][e] << 16); a[2 * e + 1] += __uint_as_float(pv[p][e] & 0xffff0000u); }
    const u32x2 o0 = {pk2(a[0], a[1]), pk2(a[2], a[3])}, o1 = {pk2(a[4], a[5]), pk2(a[6], a[7])};
    const rsrc_t ro = __builtin_amdgcn_make_buffer_rsrc(out + (size_t)(b * 64 + 4 * h) * 896, 0, 3584, 0x00020000);
    const u32 oo = (tix & 3) * 896 + (tix >> 4) * 64 + ((tix >> 2) & 3) * 8;
    __builtin_amdgcn_raw_buffer_store_b64(o0, ro, oo, 0, 0);
    __builtin_amdgcn_raw_buffer_store_b64(o1, ro, oo + 32, 0, 0);
  }
  asm volatile(".Lgqa_end_l:");
  __builtin_amdgcn_endpgm();
}

__global__ __launch_bounds__(512, 2) void gqa_block_kernel(const char* __restrict__ x, const char* __restrict__ k, const char* __restrict__ v,
                                                           const char* __restrict__ wq, const char* __restrict__ wo, char* out, char* ws) {
  __shared__ __attribute__((aligned(16))) char smem[LDS_BYTES];
  const u32 lds = (u32)(size_t)(LDSA char*)smem;
  const int bid = blockIdx.x, b = bid >> 4, hh = (bid >> 3) & 1, kvh = bid & 7, h = kvh * 2 + hh;
  const int wid = __builtin_amdgcn_readfirstlane(threadIdx.x >> 6), lane = threadIdx.x & 63, w = wid & 3;
  const u32 rot = (u32)(0x1065432106543210ull >> (4 * ((b >> 2) * 2 + hh * 3)));
  u64 pc;
  u32 len;
  asm volatile("s_getpc_b64 %0\n.Lgqa_pc:\n\ts_mov_b32 %1, .Lgqa_end_c-.Lgqa_pc\n\ts_max_i32 %1, %1, .Lgqa_end_l-.Lgqa_pc" : "=s"(pc), "=s"(len)::"scc");
  Ctx c;
  for (int hi = 0; hi < 2; ++hi) {
    const u32 row = hi * 32 + w * 8 + (lane >> 3);
    const u32 crow = ((lane & 7) ^ ((row >> 1) & 7)) << 4;
    const u32 cnat = ((lane & 7) ^ ((((row >> 1) & 1) | (((row >> 3) & 1) << 1)) << 1)) << 4;
    const u32 cacc = ((lane & 7) ^ (((row >> 1) & 3) << 1)) << 4;
    c.vx[hi] = row * 896 + crow; c.vk[hi] = row * 512 + crow;
    c.vwq[hi] = row * 8192 + cnat; c.vv[hi] = row * 512 + cacc; c.vwo[hi] = row * 896 + cacc;
  }
  c.m0base = lds + w * 1024;
  for (int s = 0; s < 4; ++s) { c.sl[s] = ((s + b) & 3) * 128; c.slo[s] = c.sl[s] * 448; c.kvs[s] = c.sl[s] | (hh << 17); }
  for (int i = 0; i < 7; ++i) { c.r7[i] = ((rot >> (4 * i)) & 7) * 128; c.r7q[i] = c.r7[i] << 12; }
  const u32 vcode = (((lane & 7) + 8 * w) * 32 + (bid >> 3)) * 64, vbox = (lane & 15) * 4, xo = b * 57344, fo = WS_FLAGS + b * 16 * BOX, ho = h * 512;
  asm volatile("" ::"v"(c.vx[0]), "v"(c.vx[1]), "v"(c.vwq[0]), "v"(c.vwq[1]), "v"(vcode), "v"(vbox), "s"(c.m0base), "s"(c.sl[0]), "s"(c.r7[0]), "s"(c.r7[1]),
               "s"(c.r7[2]), "s"(c.r7[3]), "s"(c.r7[4]), "s"(c.r7[5]), "s"(c.r7q[0]), "s"(c.r7q[1]), "s"(c.r7q[2]), "s"(c.r7q[3]), "s"(xo), "s"(fo), "s"(ho),
               "s"(h * BOX), "s"(3670016 - ho));
  SB();
  asm volatile("" ::"s"(x), "s"(k), "s"(v), "s"(wq), "s"(wo), "s"(out), "s"(ws));
  const rsrc_t rf = __builtin_amdgcn_make_buffer_rsrc(ws + fo, 0, 16 * BOX, 0x00020000);
  if (wid >= 4) role_compute(lds, w, lane, b, h, rot, ws, rf);
  const rsrc_t rc = __builtin_amdgcn_make_buffer_rsrc((void*)pc, 0, len, 0x00020000);
  c.rx = __builtin_amdgcn_make_buffer_rsrc((void*)(x + xo), 0, 57344, 0x00020000);
  c.rk = __builtin_amdgcn_make_buffer_rsrc((void*)(k + (size_t)(b * 8 + kvh) * 262144), 0, 262144, 0x00020000);
  c.rv = __builtin_amdgcn_make_buffer_rsrc((void*)(v + (size_t)(b * 8 + kvh) * 262144), 0, 262144, 0x00020000);
  c.rwq = __builtin_amdgcn_make_buffer_rsrc((void*)(wq + ho), 0, 3670016 - ho, 0x00020000);
  c.rwo = __builtin_amdgcn_make_buffer_rsrc((void*)(wo + (size_t)h * 229376), 0, 229376, 0x00020000);
  role_load(c, w, lane, b, h, ws, out, rf, rc, vcode, vbox);
}

}

extern "C" void kernel_launch(void* const* d_in, const int* in_sizes, int n_in, void* d_out, int out_size, void* d_ws, size_t ws_size,
                              hipStream_t stream) {
  gqa_block_kernel<<<256, 512, 0, stream>>>((const char*)d_in[0], (const char*)d_in[1], (const char*)d_in[2], (const char*)d_in[3],
                                            (const char*)d_in[4], (char*)d_out, (char*)d_ws);
}
```
